```python
import torch

def _softmax(x, axis=-1):
    return torch.softmax(x, dim=axis)
def _gelu(x):
    return torch.nn.functional.gelu(x, approximate="tanh")
def _silu(x):
    return torch.nn.functional.silu(x)
def _sigmoid(x):
    return torch.sigmoid(x)
def _layernorm(x, axis=-1, eps=1e-5):
    m = x.mean(dim=axis, keepdim=True)
    v = x.var(dim=axis, keepdim=True, unbiased=False)
    return (x - m) / torch.sqrt(v + eps)
def _scatter_add(tgt, idx, src, axis=0):
    return tgt.clone().index_add_(axis, idx, src)
def _segment_sum(src, seg, n):
    out = torch.zeros((n,) + src.shape[1:], dtype=src.dtype)
    return out.index_add_(0, seg, src)
def _pad(x, pw):
    flat = tuple(v for pair in reversed(pw) for v in pair)
    return torch.nn.functional.pad(x, flat)

kernel_name = 'standalone__mha_softmax__near_ridge'

def setup_inputs():

    _w = (lambda a: (lambda t: t.to(torch.bfloat16) if t.is_floating_point() else t)(torch.as_tensor(a)))
    B, H, Sq, Sk, D = 16, 32, 304, 1024, 128
    return {
        "q": _w((lambda *s: torch.randn(*s, dtype=torch.bfloat16) * 0.8)(B, H, Sq, D)),
        "k": _w((lambda *s: torch.randn(*s, dtype=torch.bfloat16) * 0.8)(B, H, Sk, D)),
        "v": _w((lambda *s: torch.randn(*s, dtype=torch.bfloat16) * 0.05)(B, H, Sk, D)),
    }

def reference(q, k, v):
    scale = q.shape[-1] ** -0.5
    scores = torch.einsum("bhqd,bhkd->bhqk", q, k) * scale
    p = _softmax(scores, axis=-1)
    return torch.einsum("bhqk,bhkd->bhqd", p, v)
```

```cpp
#include <hip/hip_runtime.h>
#include <hip/hip_bf16.h>
constexpr int B=16,H=32,SQ=304,SK=1024,D=128;
__device__ __forceinline__ float b2f(unsigned short x){ return __uint_as_float(((unsigned)x)<<16); }
__global__ __launch_bounds__(256) void naive(const unsigned short* q,const unsigned short* k,const unsigned short* v,__hip_bfloat16* o){
  __shared__ float qs[4][D]; __shared__ float ps[4][SK];
  int w=threadIdx.x>>6, lane=threadIdx.x&63;
  long row=(long)blockIdx.x*4+w;
  long bh=row/SQ;
  const unsigned short* qp=q+row*D;
  qs[w][lane]=b2f(qp[lane]); qs[w][lane+64]=b2f(qp[lane+64]);
  __syncthreads();
  const unsigned short* kp=k+bh*SK*D; const unsigned short* vp=v+bh*SK*D;
  float mx=-1e30f;
  for(int j=lane;j<SK;j+=64){ float s=0; for(int d=0;d<D;d++) s+=qs[w][d]*b2f(kp[(long)j*D+d]); s*=0.08838834764831845f; ps[w][j]=s; mx=fmaxf(mx,s);}
  for(int o_=32;o_;o_>>=1) mx=fmaxf(mx,__shfl_xor(mx,o_));
  float sum=0;
  for(int j=lane;j<SK;j+=64){ float e=__expf(ps[w][j]-mx); ps[w][j]=e; sum+=e;}
  for(int o_=32;o_;o_>>=1) sum+=__shfl_xor(sum,o_);
  __syncthreads();
  float a0=0,a1=0;
  for(int j=0;j<SK;j++){ float p=ps[w][j]; a0+=p*b2f(vp[(long)j*D+lane]); a1+=p*b2f(vp[(long)j*D+lane+64]); }
  o[row*D+lane]=__float2bfloat16(a0/sum); o[row*D+lane+64]=__float2bfloat16(a1/sum);
}
extern "C" void kernel_launch(void* const* d_in, const int* in_sizes, int n_in, void* d_out, int out_size, void* d_ws, size_t ws_size, hipStream_t stream) {
  naive<<<B*H*SQ/4,256,0,stream>>>((const unsigned short*)d_in[0],(const unsigned short*)d_in[1],(const unsigned short*)d_in[2],(__hip_bfloat16*)d_out);
}
```

```cpp
#include <hip/hip_runtime.h>
#include <hip/hip_bf16.h>

__global__ __launch_bounds__(256, 1) void mha_fwd_kernel(const void* q, const void* k, const void* v, void* o) {
  __shared__ __attribute__((aligned(1024))) char lds[160000];
  unsigned w = __builtin_amdgcn_readfirstlane(threadIdx.x >> 6);
  unsigned bid = blockIdx.x;
  unsigned ldsb = (unsigned)(size_t)lds;
  asm volatile(
    "s_mov_b64 s[88:89], %0\n"
    "s_mov_b64 s[84:85], %1\n"
    "s_mov_b64 s[86:87], %2\n"
    "s_mov_b64 s[90:91], %3\n"
    "s_mov_b32 s52, %4\n"
    "s_mov_b32 s40, %5\n"
    "s_mov_b32 s71, %6\n"
    "s_lshl_b32 s52, s52, 1\n"
    "s_mov_b32 s72, 1\n"
    "s_and_b32 s85, s85, 0xffff\n"
    "s_and_b32 s87, s87, 0xffff\n"
    "s_and_b32 s89, s89, 0xffff\n"
    "s_and_b32 s91, s91, 0xffff\n"
    "s_mov_b32 s20, s84\n"
    "s_mov_b32 s21, s85\n"
    "s_mov_b32 s23, 0x20000\n"
    "s_mov_b32 s24, s86\n"
    "s_mov_b32 s25, s87\n"
    "s_mov_b32 s27, 0x20000\n"
    "s_mov_b32 s28, s90\n"
    "s_mov_b32 s29, s91\n"
    "s_mov_b32 s30, 0\n"
    "s_mov_b32 s31, 0x20000\n"
    "s_mov_b32 s39, 0x20000\n"
    "s_mul_i32 s41, s40, 4352\n"
    "s_add_u32 s41, s41, s71\n"
    "s_mul_i32 s54, s40, 20480\n"
    "s_mul_i32 s81, s40, 21760\n"
    "s_add_u32 s81, s81, 69632\n"
    "s_add_u32 s56, s71, 156672\n"
    "s_mul_i32 s42, s52, 262144\n"
    "s_lshl_b32 s62, s40, 12\n"
    "s_add_u32 s42, s42, s62\n"
    "s_add_u32 s43, s42, 524288\n"
    "v_mbcnt_lo_u32_b32 v249, -1, 0\n"
    "v_mbcnt_hi_u32_b32 v249, -1, v249\n"
    "v_lshrrev_b32 v245, 4, v249\n"
    "v_and_b32 v244, 15, v249\n"
    "v_xor_b32 v244, v244, v245\n"
    "v_lshlrev_b32 v244, 4, v244\n"
    "v_lshl_add_u32 v237, v245, 8, v244\n"
    "v_bfe_u32 v244, v249, 2, 2\n"
    "v_lshlrev_b32 v244, 8, v244\n"
    "v_lshl_add_u32 v244, v245, 6, v244\n"
    "v_and_b32 v243, 3, v249\n"
    "v_lshl_add_u32 v238, v243, 4, v244\n"
    "s_mul_i32 s62, s52, 77824\n"
    "s_add_u32 s36, s88, s62\n"
    "s_addc_u32 s37, s89, 0\n"
    "s_mov_b32 s38, 77824\n"
    "s_add_u32 s64, s81, s71\n"
    "s_mov_b32 s44, s54\n"
    "s_add_u32 s65, s54, 0\n"
    "s_add_u32 m0, s64, 0\n"
    "s_nop 0\n"
    "buffer_load_dwordx4 v237, s[36:39], s65 offen offset:0 nt lds\n"
    "s_add_u32 m0, s64, 64\n"
    "s_nop 0\n"
    "buffer_load_dwordx4 v237, s[36:39], s65 offen offset:1024 nt lds\n"
    "s_add_u32 m0, s64, 128\n"
    "s_nop 0\n"
    "buffer_load_dwordx4 v237, s[36:39], s65 offen offset:2048 nt lds\n"
    "s_add_u32 m0, s64, 192\n"
    "s_nop 0\n"
    "buffer_load_dwordx4 v237, s[36:39], s65 offen offset:3072 nt lds\n"
    "s_cmp_lt_u32 s42, s43\n"
    "s_cselect_b32 s22, 134217728, 0\n"
    "s_mov_b32 s26, s22\n"
    "s_add_u32 s45, 0, s41\n"
    "s_add_u32 s46, s45, 17408\n"
    "s_mov_b32 m0, s45\n"
    "s_nop 0\n"
    "buffer_load_dwordx4 v237, s[20:23], s42 offen offset:0 nt lds\n"
    "s_add_u32 m0, m0, 64\n"
    "s_nop 0\n"
    "buffer_load_dwordx4 v237, s[20:23], s42 offen offset:1024 nt lds\n"
    "s_add_u32 m0, m0, 64\n"
    "s_nop 0\n"
    "buffer_load_dwordx4 v237, s[20:23], s42 offen offset:2048 nt lds\n"
    "s_add_u32 m0, m0, 64\n"
    "s_nop 0\n"
    "buffer_load_dwordx4 v237, s[20:23], s42 offen offset:3072 nt lds\n"
    "s_add_u32 s65, s54, 4096\n"
    "s_add_u32 m0, s64, 4352\n"
    "s_nop 0\n"
    "buffer_load_dwordx4 v237, s[36:39], s65 offen offset:0 nt lds\n"
    "s_add_u32 m0, s64, 4416\n"
    "s_nop 0\n"
    "buffer_load_dwordx4 v237, s[36:39], s65 offen offset:1024 nt lds\n"
    "s_add_u32 m0, s64, 4480\n"
    "s_nop 0\n"
    "buffer_load_dwordx4 v237, s[36:39], s65 offen offset:2048 nt lds\n"
    "s_add_u32 m0, s64, 4544\n"
    "s_nop 0\n"
    "buffer_load_dwordx4 v237, s[36:39], s65 offen offset:3072 nt lds\n"
    "s_mov_b32 s47, 0\n"
    "s_mov_b32 s48, 34816\n"
    "s_mov_b32 s53, 0x47800000\n"
    "s_mov_b32 s73, 0xffff0000\n"
    "s_mov_b32 s58, 0x3e0293ee\n"
    "s_mov_b32 s59, 0x3e0293ee\n"
    "s_mov_b32 s61, 0x7f80\n"
    "s_mov_b32 s70, 0x2924\n"
    "v_bfe_u32 v243, v249, 2, 2\n"
    "v_mul_u32_u24 v243, 1088, v243\n"
    "v_and_b32 v244, 3, v249\n"
    "v_lshl_add_u32 v243, v244, 8, v243\n"
    "v_xor_b32 v244, v244, v245\n"
    "v_lshl_add_u32 v243, v244, 4, v243\n"
    "v_add_u32 v241, s71, v243\n"
    "v_mul_u32_u24 v243, 1056, v245\n"
    "v_bfe_u32 v244, v249, 2, 2\n"
    "v_lshl_add_u32 v243, v244, 6, v243\n"
    "v_and_b32 v244, 3, v249\n"
    "v_lshl_add_u32 v243, v244, 3, v243\n"
    "s_add_u32 s62, s71, 17408\n"
    "v_add_u32 v236, s62, v243\n"
    "v_and_b32 v243, 15, v249\n"
    "v_mul_u32_u24 v244, 272, v243\n"
    "v_lshl_add_u32 v244, v245, 3, v244\n"
    "s_add_u32 s62, s81, s71\n"
    "v_add_u32 v235, s62, v244\n"
    "v_mul_u32_u24 v244, 272, v245\n"
    "v_lshl_add_u32 v239, v243, 4, v244\n"
    "v_lshlrev_b32 v240, 4, v249\n"
    "v_mov_b32 v243, 0x3f803f80\n"
    "s_nop 0\n"
    "v_accvgpr_write_b32 a240, v243\n"
    "v_accvgpr_write_b32 a241, v243\n"
    "v_accvgpr_write_b32 a242, v243\n"
    "v_accvgpr_write_b32 a243, v243\n"
    "s_lshl_b32 s62, s40, 8\n"
    "s_add_u32 s62, s62, 158976\n"
    "s_add_u32 s62, s62, s71\n"
    "v_lshlrev_b32 v243, 2, v249\n"
    "v_add_u32 v254, s62, v243\n"
    "v_and_b32 v243, 3, v249\n"
    "v_lshlrev_b32 v243, 8, v243\n"
    "s_add_u32 s62, s71, 158976\n"
    "v_add_u32 v255, s62, v243\n"
    "v_mov_b32 v253, 0\n"
    "s_nop 0\n"
    "ds_write_b32 v254, v253 offset:0\n"
    "v_mov_b32 v224, 0\n"
    "v_mov_b32 v225, 0\n"
    "v_mov_b32 v226, 0\n"
    "v_mov_b32 v227, 0\n"
    "s_cmp_eq_u32 s40, 3\n"
    "s_cselect_b32 s63, 0x46ea6000, 0\n"
    "s_nop 0\n"
    "v_mfma_f32_32x32x16_bf16 v[0:15], v[224:227], v[224:227], 0\n"
    "v_mfma_f32_32x32x16_bf16 v[16:31], v[224:227], v[224:227], 0\n"
    "v_mfma_f32_32x32x16_bf16 v[32:47], v[224:227], v[224:227], 0\n"
    "v_mfma_f32_32x32x16_bf16 v[48:63], v[224:227], v[224:227], 0\n"
    "v_mfma_f32_32x32x16_bf16 v[64:79], v[224:227], v[224:227], 0\n"
    "v_mfma_f32_32x32x16_bf16 v[80:95], v[224:227], v[224:227], 0\n"
    "v_mfma_f32_32x32x16_bf16 v[96:111], v[224:227], v[224:227], 0\n"
    "v_mfma_f32_32x32x16_bf16 v[112:127], v[224:227], v[224:227], 0\n"
    "v_mfma_f32_32x32x16_bf16 a[0:15], v[224:227], v[224:227], 0\n"
    "v_mfma_f32_32x32x16_bf16 a[16:31], v[224:227], v[224:227], 0\n"
    "v_mfma_f32_32x32x16_bf16 a[176:191], v[224:227], v[224:227], 0\n"
    "v_mfma_f32_32x32x16_bf16 a[192:207], v[224:227], v[224:227], 0\n"
    "v_mfma_f32_32x32x16_bf16 a[208:223], v[224:227], v[224:227], 0\n"
    "v_mfma_f32_32x32x16_bf16 a[224:239], v[224:227], v[224:227], 0\n"
    "s_add_u32 s65, s54, 8192\n"
    "s_add_u32 m0, s64, 8704\n"
    "s_nop 0\n"
    "buffer_load_dwordx4 v237, s[36:39], s65 offen offset:0 nt lds\n"
    "s_add_u32 m0, s64, 8768\n"
    "s_nop 0\n"
    "buffer_load_dwordx4 v237, s[36:39], s65 offen offset:1024 nt lds\n"
    "s_add_u32 m0, s64, 8832\n"
    "s_nop 0\n"
    "buffer_load_dwordx4 v237, s[36:39], s65 offen offset:2048 nt lds\n"
    "s_add_u32 m0, s64, 8896\n"
    "s_nop 0\n"
    "buffer_load_dwordx4 v237, s[36:39], s65 offen offset:3072 nt lds\n"
    "s_waitcnt vmcnt(12)\n"
    "v_add_u32 v244, s81, v241\n"
    "ds_read_b128 v[128:131], v244 offset:0\n"
    "ds_read_b128 v[132:135], v244 offset:64\n"
    "ds_read_b128 v[136:139], v244 offset:128\n"
    "ds_read_b128 v[140:143], v244 offset:192\n"
    "s_waitcnt lgkmcnt(0)\n"
    "v_lshlrev_b32 v192, 16, v128\n"
    "v_and_b32 v193, s73, v128\n"
    "v_lshlrev_b32 v194, 16, v129\n"
    "v_and_b32 v195, s73, v129\n"
    "v_lshlrev_b32 v196, 16, v130\n"
    "v_and_b32 v197, s73, v130\n"
    "v_lshlrev_b32 v198, 16, v131\n"
    "v_and_b32 v199, s73, v131\n"
    "v_pk_mul_f32 v[192:193], v[192:193], s[58:59]\n"
    "v_pk_mul_f32 v[194:195], v[194:195], s[58:59]\n"
    "v_pk_mul_f32 v[196:197], v[196:197], s[58:59]\n"
    "v_pk_mul_f32 v[198:199], v[198:199], s[58:59]\n"
    "v_cvt_pk_bf16_f32 v128, v192, v193\n"
    "v_cvt_pk_bf16_f32 v129, v194, v195\n"
    "v_cvt_pk_bf16_f32 v130, v196, v197\n"
    "v_cvt_pk_bf16_f32 v131, v198, v199\n"
    "v_accvgpr_write_b32 a32, v128\n"
    "v_accvgpr_write_b32 a33, v129\n"
    "v_accvgpr_write_b32 a34, v130\n"
    "v_accvgpr_write_b32 a35, v131\n"
    "v_lshlrev_b32 v192, 16, v132\n"
    "v_and_b32 v193, s73, v132\n"
    "v_lshlrev_b32 v194, 16, v133\n"
    "v_and_b32 v195, s73, v133\n"
    "v_lshlrev_b32 v196, 16, v134\n"
    "v_and_b32 v197, s73, v134\n"
    "v_lshlrev_b32 v198, 16, v135\n"
    "v_and_b32 v199, s73, v135\n"
    "v_pk_mul_f32 v[192:193], v[192:193], s[58:59]\n"
    "v_pk_mul_f32 v[194:195], v[194:195], s[58:59]\n"
    "v_pk_mul_f32 v[196:197], v[196:197], s[58:59]\n"
    "v_pk_mul_f32 v[198:199], v[198:199], s[58:59]\n"
    "v_cvt_pk_bf16_f32 v132, v192, v193\n"
    "v_cvt_pk_bf16_f32 v133, v194, v195\n"
    "v_cvt_pk_bf16_f32 v134, v196, v197\n"
    "v_cvt_pk_bf16_f32 v135, v198, v199\n"
    "v_accvgpr_write_b32 a36, v132\n"
    "v_accvgpr_write_b32 a37, v133\n"
    "v_accvgpr_write_b32 a38, v134\n"
    "v_accvgpr_write_b32 a39, v135\n"
    "v_lshlrev_b32 v192, 16, v136\n"
    "v_and_b32 v193, s73, v136\n"
    "v_lshlrev_b32 v194, 16, v137\n"
    "v_and_b32 v195, s73, v137\n"
    "v_lshlrev_b32 v196, 16, v138\n"
    "v_and_b32 v197, s73, v138\n"
    "v_lshlrev_b32 v198, 16, v139\n"
    "v_and_b32 v199, s73, v139\n"
    "v_pk_mul_f32 v[192:193], v[192:193], s[58:59]\n"
    "v_pk_mul_f32 v[194:195], v[194:195], s[58:59]\n"
    "v_pk_mul_f32 v[196:197], v[196:197], s[58:59]\n"
    "v_pk_mul_f32 v[198:199], v[198:199], s[58:59]\n"
    "v_cvt_pk_bf16_f32 v136, v192, v193\n"
    "v_cvt_pk_bf16_f32 v137, v194, v195\n"
    "v_cvt_pk_bf16_f32 v138, v196, v197\n"
    "v_cvt_pk_bf16_f32 v139, v198, v199\n"
    "v_accvgpr_write_b32 a40, v136\n"
    "v_accvgpr_write_b32 a41, v137\n"
    "v_accvgpr_write_b32 a42, v138\n"
    "v_accvgpr_write_b32 a43, v139\n"
    "v_lshlrev_b32 v192, 16, v140\n"
    "v_and_b32 v193, s73, v140\n"
    "v_lshlrev_b32 v194, 16, v141\n"
    "v_and_b32 v195, s73, v141\n"
    "v_lshlrev_b32 v196, 16, v142\n"
    "v_and_b32 v197, s73, v142\n"
    "v_lshlrev_b32 v198, 16, v143\n"
    "v_and_b32 v199, s73, v143\n"
    "v_pk_mul_f32 v[192:193], v[192:193], s[58:59]\n"
    "v_pk_mul_f32 v[194:195], v[194:195], s[58:59]\n"
    "v_pk_mul_f32 v[196:197], v[196:197], s[58:59]\n"
    "v_pk_mul_f32 v[198:199], v[198:199], s[58:59]\n"
    "v_cvt_pk_bf16_f32 v140, v192, v193\n"
    "v_cvt_pk_bf16_f32 v141, v194, v195\n"
    "v_cvt_pk_bf16_f32 v142, v196, v197\n"
    "v_cvt_pk_bf16_f32 v143, v198, v199\n"
    "v_accvgpr_write_b32 a44, v140\n"
    "v_accvgpr_write_b32 a45, v141\n"
    "v_accvgpr_write_b32 a46, v142\n"
    "v_accvgpr_write_b32 a47, v143\n"
    "s_add_u32 s65, s54, 12288\n"
    "s_add_u32 m0, s64, 13056\n"
    "s_nop 0\n"
    "buffer_load_dwordx4 v237, s[36:39], s65 offen offset:0 nt lds\n"
    "s_add_u32 m0, s64, 13120\n"
    "s_nop 0\n"
    "buffer_load_dwordx4 v237, s[36:39], s65 offen offset:1024 nt lds\n"
    "s_add_u32 m0, s64, 13184\n"
    "s_nop 0\n"
    "buffer_load_dwordx4 v237, s[36:39], s65 offen offset:2048 nt lds\n"
    "s_add_u32 m0, s64, 13248\n"
    "s_nop 0\n"
    "buffer_load_dwordx4 v237, s[36:39], s65 offen offset:3072 nt lds\n"
    "s_waitcnt vmcnt(12)\n"
    "s_barrier\n"
    "v_add_u32 v233, s47, v241\n"
    "ds_read_b128 a[112:115], v233 offset:0\n"
    "ds_read_b128 a[116:119], v233 offset:64\n"
    "ds_read_b128 a[120:123], v233 offset:128\n"
    "ds_read_b128 a[124:127], v233 offset:192\n"
    "ds_read_b128 a[128:131], v233 offset:4352\n"
    "ds_read_b128 a[132:135], v233 offset:4416\n"
    "ds_read_b128 a[136:139], v233 offset:4480\n"
    "ds_read_b128 a[140:143], v233 offset:4544\n"
    "ds_read_b128 a[144:147], v233 offset:8704\n"
    "ds_read_b128 a[148:151], v233 offset:8768\n"
    "ds_read_b128 a[152:155], v233 offset:8832\n"
    "ds_read_b128 a[156:159], v233 offset:8896\n"
    "ds_read_b128 a[160:163], v233 offset:13056\n"
    "ds_read_b128 a[164:167], v233 offset:13120\n"
    "ds_read_b128 a[168:171], v233 offset:13184\n"
    "ds_read_b128 a[172:175], v233 offset:13248\n"
    "s_waitcnt lgkmcnt(0)\n"
    "s_barrier\n"
    "s_waitcnt vmcnt(8)\n"
    "ds_read_b128 v[144:147], v244 offset:4352\n"
    "v_mfma_f32_16x16x32_bf16 v[200:203], a[112:115], a[32:35], 0\n"
    "ds_read_b128 v[148:151], v244 offset:4416\n"
    "ds_read_b128 v[152:155], v244 offset:4480\n"
    "v_mfma_f32_16x16x32_bf16 v[200:203], a[116:119], a[36:39], v[200:203]\n"
    "ds_read_b128 v[156:159], v244 offset:4544\n"
    "s_add_u32 s65, s54, 16384\n"
    "s_add_u32 m0, s64, 17408\n"
    "v_mfma_f32_16x16x32_bf16 v[200:203], a[120:123], a[40:43], v[200:203]\n"
    "s_nop 0\n"
    "buffer_load_dwordx4 v237, s[36:39], s65 offen offset:0 nt lds\n"
    "v_mfma_f32_16x16x32_bf16 v[200:203], a[124:127], a[44:47], v[200:203]\n"
    "s_add_u32 m0, s64, 17472\n"
    "s_nop 0\n"
    "buffer_load_dwordx4 v237, s[36:39], s65 offen offset:1024 nt lds\n"
    "s_add_u32 m0, s64, 17536\n"
    "s_nop 0\n"
    "buffer_load_dwordx4 v237, s[36:39], s65 offen offset:2048 nt lds\n"
    "s_add_u32 m0, s64, 17600\n"
    "s_nop 0\n"
    "buffer_load_dwordx4 v237, s[36:39], s65 offen offset:3072 nt lds\n"
    "s_waitcnt lgkmcnt(0)\n"
    "v_lshlrev_b32 v192, 16, v144\n"
    "v_and_b32 v193, s73, v144\n"
    "v_lshlrev_b32 v194, 16, v145\n"
    "v_and_b32 v195, s73, v145\n"
    "v_lshlrev_b32 v196, 16, v146\n"
    "v_max3_f32 v245, v200, v201, v202\n"
    "v_and_b32 v197, s73, v146\n"
    "v_lshlrev_b32 v198, 16, v147\n"
    "v_and_b32 v199, s73, v147\n"
    "v_max_f32 v245, v245, v203\n"
    "v_pk_mul_f32 v[192:193], v[192:193], s[58:59]\n"
    "v_pk_mul_f32 v[194:195], v[194:195], s[58:59]\n"
    "v_pk_mul_f32 v[196:197], v[196:197], s[58:59]\n"
    "v_mov_b32 v246, v245\n"
    "v_pk_mul_f32 v[198:199], v[198:199], s[58:59]\n"
    "v_cvt_pk_bf16_f32 v144, v192, v193\n"
    "v_cvt_pk_bf16_f32 v145, v194, v195\n"
    "v_permlane16_swap_b32 v245, v246\n"
    "v_cvt_pk_bf16_f32 v146, v196, v197\n"
    "v_cvt_pk_bf16_f32 v147, v198, v199\n"
    "v_accvgpr_write_b32 a48, v144\n"
    "v_max_f32 v245, v245, v246\n"
    "v_accvgpr_write_b32 a49, v145\n"
    "v_accvgpr_write_b32 a50, v146\n"
    "v_accvgpr_write_b32 a51, v147\n"
    "v_mov_b32 v246, v245\n"
    "v_lshlrev_b32 v192, 16, v148\n"
    "v_and_b32 v193, s73, v148\n"
    "v_lshlrev_b32 v194, 16, v149\n"
    "v_permlane32_swap_b32 v245, v246\n"
    "v_and_b32 v195, s73, v149\n"
    "v_lshlrev_b32 v196, 16, v150\n"
    "v_and_b32 v197, s73, v150\n"
    "v_max_f32 v245, v245, v246\n"
    "v_lshlrev_b32 v198, 16, v151\n"
    "v_and_b32 v199, s73, v151\n"
    "v_pk_mul_f32 v[192:193], v[192:193], s[58:59]\n"
    "v_sub_f32 v200, 0, v245\n"
    "v_pk_mul_f32 v[194:195], v[194:195], s[58:59]\n"
    "v_pk_mul_f32 v[196:197], v[196:197], s[58:59]\n"
    "v_pk_mul_f32 v[198:199], v[198:199], s[58:59]\n"
    "v_sub_f32 v201, 0, v245\n"
    "v_cvt_pk_bf16_f32 v148, v192, v193\n"
    "v_cvt_pk_bf16_f32 v149, v194, v195\n"
    "v_cvt_pk_bf16_f32 v150, v196, v197\n"
    "v_sub_f32 v202, 0, v245\n"
    "v_cvt_pk_bf16_f32 v151, v198, v199\n"
    "v_accvgpr_write_b32 a52, v148\n"
    "v_accvgpr_write_b32 a53, v149\n"
    "v_sub_f32 v203, 0, v245\n"
    "v_accvgpr_write_b32 a54, v150\n"
    "v_accvgpr_write_b32 a55, v151\n"
    "v_lshlrev_b32 v192, 16, v152\n"
    "v_and_b32 v193, s73, v152\n"
    "v_lshlrev_b32 v194, 16, v153\n"
    "v_and_b32 v195, s73, v153\n"
    "v_lshlrev_b32 v196, 16, v154\n"
    "v_and_b32 v197, s73, v154\n"
    "v_lshlrev_b32 v198, 16, v155\n"
    "v_and_b32 v199, s73, v155\n"
    "v_pk_mul_f32 v[192:193], v[192:193], s[58:59]\n"
    "v_pk_mul_f32 v[194:195], v[194:195], s[58:59]\n"
    "v_pk_mul_f32 v[196:197], v[196:197], s[58:59]\n"
    "v_pk_mul_f32 v[198:199], v[198:199], s[58:59]\n"
    "v_cvt_pk_bf16_f32 v152, v192, v193\n"
    "v_cvt_pk_bf16_f32 v153, v194, v195\n"
    "v_cvt_pk_bf16_f32 v154, v196, v197\n"
    "v_cvt_pk_bf16_f32 v155, v198, v199\n"
    "v_accvgpr_write_b32 a56, v152\n"
    "v_accvgpr_write_b32 a57, v153\n"
    "v_accvgpr_write_b32 a58, v154\n"
    "v_accvgpr_write_b32 a59, v155\n"
    "v_lshlrev_b32 v192, 16, v156\n"
    "v_and_b32 v193, s73, v156\n"
    "v_lshlrev_b32 v194, 16, v157\n"
    "v_and_b32 v195, s73, v157\n"
    "v_lshlrev_b32 v196, 16, v158\n"
    "v_and_b32 v197, s73, v158\n"
    "v_lshlrev_b32 v198, 16, v159\n"
    "v_and_b32 v199, s73, v159\n"
    "v_pk_mul_f32 v[192:193], v[192:193], s[58:59]\n"
    "v_pk_mul_f32 v[194:195], v[194:195], s[58:59]\n"
    "v_pk_mul_f32 v[196:197], v[196:197], s[58:59]\n"
    "v_pk_mul_f32 v[198:199], v[198:199], s[58:59]\n"
    "v_cvt_pk_bf16_f32 v156, v192, v193\n"
    "v_cvt_pk_bf16_f32 v157, v194, v195\n"
    "v_cvt_pk_bf16_f32 v158, v196, v197\n"
    "v_cvt_pk_bf16_f32 v159, v198, v199\n"
    "v_accvgpr_write_b32 a60, v156\n"
    "v_accvgpr_write_b32 a61, v157\n"
    "v_accvgpr_write_b32 a62, v158\n"
    "v_accvgpr_write_b32 a63, v159\n"
    "v_mfma_f32_16x16x32_bf16 v[128:131], a[112:115], a[32:35], v[200:203]\n"
    "s_mov_b32 m0, s46\n"
    "s_nop 0\n"
    "v_mfma_f32_16x16x32_bf16 v[204:207], a[112:115], a[48:51], 0\n"
    "v_mfma_f32_16x16x32_bf16 v[128:131], a[116:119], a[36:39], v[128:131]\n"
    "buffer_load_dwordx4 v238, s[24:27], s42 offen offset:0 nt lds\n"
    "s_add_u32 m0, m0, 32\n"
    "v_mfma_f32_16x16x32_bf16 v[204:207], a[116:119], a[52:55], v[204:207]\n"
    "v_mfma_f32_16x16x32_bf16 v[128:131], a[120:123], a[40:43], v[128:131]\n"
    "s_nop 0\n"
    "buffer_load_dwordx4 v238, s[24:27], s42 offen offset:1024 nt lds\n"
    "v_mfma_f32_16x16x32_bf16 v[204:207], a[120:123], a[56:59], v[204:207]\n"
    "v_mfma_f32_16x16x32_bf16 v[128:131], a[124:127], a[44:47], v[128:131]\n"
    "s_add_u32 m0, m0, 32\n"
    "s_nop 0\n"
    "v_mfma_f32_16x16x32_bf16 v[204:207], a[124:127], a[60:63], v[204:207]\n"
    "v_mfma_f32_16x16x32_bf16 v[132:135], a[128:131], a[32:35], v[200:203]\n"
    "buffer_load_dwordx4 v238, s[24:27], s42 offen offset:2048 nt lds\n"
    "s_add_u32 m0, m0, 32\n"
    "v_mfma_f32_16x16x32_bf16 v[132:135], a[132:135], a[36:39], v[132:135]\n"
    "s_nop 0\n"
    "buffer_load_dwordx4 v238, s[24:27], s42 offen offset:3072 nt lds\n"
    "v_mfma_f32_16x16x32_bf16 v[132:135], a[136:139], a[40:43], v[132:135]\n"
    "s_add_u32 s42, s42, 0x4000\n"
    "s_waitcnt vmcnt(12)\n"
    "v_mfma_f32_16x16x32_bf16 v[132:135], a[140:143], a[44:47], v[132:135]\n"
    "ds_read_b128 v[160:163], v244 offset:8704\n"
    "ds_read_b128 v[164:167], v244 offset:8768\n"
    "v_mfma_f32_16x16x32_bf16 v[136:139], a[144:147], a[32:35], v[200:203]\n"
    "ds_read_b128 v[168:171], v244 offset:8832\n"
    "ds_read_b128 v[172:175], v244 offset:8896\n"
    "v_mfma_f32_16x16x32_bf16 v[136:139], a[148:151], a[36:39], v[136:139]\n"
    "s_waitcnt lgkmcnt(0)\n"
    "v_lshlrev_b32 v192, 16, v160\n"
    "v_mfma_f32_16x16x32_bf16 v[136:139], a[152:155], a[40:43], v[136:139]\n"
    "v_and_b32 v193, s73, v160\n"
    "v_lshlrev_b32 v194, 16, v161\n"
    "v_mfma_f32_16x16x32_bf16 v[136:139], a[156:159], a[44:47], v[136:139]\n"
    "v_and_b32 v195, s73, v161\n"
    "v_lshlrev_b32 v196, 16, v162\n"
    "v_max3_f32 v247, v204, v205, v206\n"
    "v_mfma_f32_16x16x32_bf16 v[140:143], a[160:163], a[32:35], v[200:203]\n"
    "v_and_b32 v197, s73, v162\n"
    "v_lshlrev_b32 v198, 16, v163\n"
    "v_mfma_f32_16x16x32_bf16 v[140:143], a[164:167], a[36:39], v[140:143]\n"
    "v_and_b32 v199, s73, v163\n"
    "v_max_f32 v247, v247, v207\n"
    "v_pk_mul_f32 v[192:193], v[192:193], s[58:59]\n"
    "v_mfma_f32_16x16x32_bf16 v[140:143], a[168:171], a[40:43], v[140:143]\n"
    "v_pk_mul_f32 v[194:195], v[194:195], s[58:59]\n"
    "v_pk_mul_f32 v[196:197], v[196:197], s[58:59]\n"
    "v_mov_b32 v248, v247\n"
    "v_mfma_f32_16x16x32_bf16 v[140:143], a[172:175], a[44:47], v[140:143]\n"
    "v_pk_mul_f32 v[198:199], v[198:199], s[58:59]\n"
    "v_cvt_pk_bf16_f32 v160, v192, v193\n"
    "v_cvt_pk_bf16_f32 v161, v194, v195\n"
    "v_permlane16_swap_b32 v247, v248\n"
    "v_cvt_pk_bf16_f32 v162, v196, v197\n"
    "v_cvt_pk_bf16_f32 v163, v198, v199\n"
    "v_accvgpr_write_b32 a64, v160\n"
    "v_max_f32 v247, v247, v248\n"
    "v_accvgpr_write_b32 a65, v161\n"
    "v_accvgpr_write_b32 a66, v162\n"
    "v_accvgpr_write_b32 a67, v163\n"
    "v_mov_b32 v248, v247\n"
    "v_lshlrev_b32 v192, 16, v164\n"
    "v_and_b32 v193, s73, v164\n"
    "v_lshlrev_b32 v194, 16, v165\n"
    "v_permlane32_swap_b32 v247, v248\n"
    "v_and_b32 v195, s73, v165\n"
    "v_lshlrev_b32 v196, 16, v166\n"
    "v_and_b32 v197, s73, v166\n"
    "v_max_f32 v247, v247, v248\n"
    "v_lshlrev_b32 v198, 16, v167\n"
    "v_and_b32 v199, s73, v167\n"
    "v_pk_mul_f32 v[192:193], v[192:193], s[58:59]\n"
    "v_sub_f32 v204, 0, v247\n"
    "v_pk_mul_f32 v[194:195], v[194:195], s[58:59]\n"
    "v_pk_mul_f32 v[196:197], v[196:197], s[58:59]\n"
    "v_pk_mul_f32 v[198:199], v[198:199], s[58:59]\n"
    "v_sub_f32 v205, 0, v247\n"
    "v_cvt_pk_bf16_f32 v164, v192, v193\n"
    "v_cvt_pk_bf16_f32 v165, v194, v195\n"
    "v_cvt_pk_bf16_f32 v166, v196, v197\n"
    "v_sub_f32 v206, 0, v247\n"
    "v_cvt_pk_bf16_f32 v167, v198, v199\n"
    "v_accvgpr_write_b32 a68, v164\n"
    "v_accvgpr_write_b32 a69, v165\n"
    "v_sub_f32 v207, 0, v247\n"
    "v_accvgpr_write_b32 a70, v166\n"
    "v_accvgpr_write_b32 a71, v167\n"
    "v_lshlrev_b32 v192, 16, v168\n"
    "v_and_b32 v193, s73, v168\n"
    "v_lshlrev_b32 v194, 16, v169\n"
    "v_and_b32 v195, s73, v169\n"
    "v_lshlrev_b32 v196, 16, v170\n"
    "v_and_b32 v197, s73, v170\n"
    "v_lshlrev_b32 v198, 16, v171\n"
    "v_and_b32 v199, s73, v171\n"
    "v_pk_mul_f32 v[192:193], v[192:193], s[58:59]\n"
    "v_pk_mul_f32 v[194:195], v[194:195], s[58:59]\n"
    "v_pk_mul_f32 v[196:197], v[196:197], s[58:59]\n"
    "v_pk_mul_f32 v[198:199], v[198:199], s[58:59]\n"
    "v_cvt_pk_bf16_f32 v168, v192, v193\n"
    "v_cvt_pk_bf16_f32 v169, v194, v195\n"
    "v_cvt_pk_bf16_f32 v170, v196, v197\n"
    "v_cvt_pk_bf16_f32 v171, v198, v199\n"
    "v_accvgpr_write_b32 a72, v168\n"
    "v_accvgpr_write_b32 a73, v169\n"
    "v_accvgpr_write_b32 a74, v170\n"
    "v_accvgpr_write_b32 a75, v171\n"
    "v_lshlrev_b32 v192, 16, v172\n"
    "v_and_b32 v193, s73, v172\n"
    "v_lshlrev_b32 v194, 16, v173\n"
    "v_and_b32 v195, s73, v173\n"
    "v_lshlrev_b32 v196, 16, v174\n"
    "v_and_b32 v197, s73, v174\n"
    "v_lshlrev_b32 v198, 16, v175\n"
    "v_and_b32 v199, s73, v175\n"
    "v_pk_mul_f32 v[192:193], v[192:193], s[58:59]\n"
    "v_pk_mul_f32 v[194:195], v[194:195], s[58:59]\n"
    "v_pk_mul_f32 v[196:197], v[196:197], s[58:59]\n"
    "v_pk_mul_f32 v[198:199], v[198:199], s[58:59]\n"
    "v_cvt_pk_bf16_f32 v172, v192, v193\n"
    "v_cvt_pk_bf16_f32 v173, v194, v195\n"
    "v_cvt_pk_bf16_f32 v174, v196, v197\n"
    "v_cvt_pk_bf16_f32 v175, v198, v199\n"
    "v_accvgpr_write_b32 a76, v172\n"
    "v_accvgpr_write_b32 a77, v173\n"
    "v_accvgpr_write_b32 a78, v174\n"
    "v_accvgpr_write_b32 a79, v175\n"
    "s_cmp_lt_u32 s42, s43\n"
    "s_cselect_b32 s22, 134217728, 0\n"
    "s_mov_b32 s26, s22\n"
    "s_add_u32 s45, s48, s41\n"
    "s_add_u32 s46, s45, 17408\n"
    "s_mov_b32 m0, s45\n"
    "v_mfma_f32_16x16x32_bf16 v[208:211], a[112:115], a[64:67], 0\n"
    "s_nop 0\n"
    "buffer_load_dwordx4 v237, s[20:23], s42 offen offset:0 nt lds\n"
    "v_mfma_f32_16x16x32_bf16 v[208:211], a[116:119], a[68:71], v[208:211]\n"
    "s_add_u32 m0, m0, 64\n"
    "s_nop 0\n"
    "v_mfma_f32_16x16x32_bf16 v[208:211], a[120:123], a[72:75], v[208:211]\n"
    "buffer_load_dwordx4 v237, s[20:23], s42 offen offset:1024 nt lds\n"
    "s_add_u32 m0, m0, 64\n"
    "v_mfma_f32_16x16x32_bf16 v[208:211], a[124:127], a[76:79], v[208:211]\n"
    "s_nop 0\n"
    "buffer_load_dwordx4 v237, s[20:23], s42 offen offset:2048 nt lds\n"
    "s_add_u32 m0, m0, 64\n"
    "s_nop 0\n"
    "buffer_load_dwordx4 v237, s[20:23], s42 offen offset:3072 nt lds\n"
    "s_waitcnt vmcnt(12)\n"
    "ds_read_b128 v[176:179], v244 offset:13056\n"
    "ds_read_b128 v[180:183], v244 offset:13120\n"
    "ds_read_b128 v[184:187], v244 offset:13184\n"
    "ds_read_b128 v[188:191], v244 offset:13248\n"
    "s_waitcnt lgkmcnt(0)\n"
    "v_lshlrev_b32 v192, 16, v176\n"
    "v_max3_f32 v249, v208, v209, v210\n"
    "v_and_b32 v193, s73, v176\n"
    "v_lshlrev_b32 v194, 16, v177\n"
    "v_and_b32 v195, s73, v177\n"
    "v_max_f32 v249, v249, v211\n"
    "v_lshlrev_b32 v196, 16, v178\n"
    "v_and_b32 v197, s73, v178\n"
    "v_lshlrev_b32 v198, 16, v179\n"
    "v_mov_b32 v250, v249\n"
    "v_and_b32 v199, s73, v179\n"
    "v_pk_mul_f32 v[192:193], v[192:193], s[58:59]\n"
    "v_pk_mul_f32 v[194:195], v[194:195], s[58:59]\n"
    "v_permlane16_swap_b32 v249, v250\n"
    "v_pk_mul_f32 v[196:197], v[196:197], s[58:59]\n"
    "v_pk_mul_f32 v[198:199], v[198:199], s[58:59]\n"
    "v_cvt_pk_bf16_f32 v176, v192, v193\n"
    "v_max_f32 v249, v249, v250\n"
    "v_cvt_pk_bf16_f32 v177, v194, v195\n"
    "v_cvt_pk_bf16_f32 v178, v196, v197\n"
    "v_cvt_pk_bf16_f32 v179, v198, v199\n"
    "v_mov_b32 v250, v249\n"
    "v_accvgpr_write_b32 a80, v176\n"
    "v_accvgpr_write_b32 a81, v177\n"
    "v_accvgpr_write_b32 a82, v178\n"
    "v_permlane32_swap_b32 v249, v250\n"
    "v_accvgpr_write_b32 a83, v179\n"
    "v_lshlrev_b32 v192, 16, v180\n"
    "v_and_b32 v193, s73, v180\n"
    "v_max_f32 v249, v249, v250\n"
    "v_lshlrev_b32 v194, 16, v181\n"
    "v_and_b32 v195, s73, v181\n"
    "v_lshlrev_b32 v196, 16, v182\n"
    "v_sub_f32 v208, 0, v249\n"
    "v_and_b32 v197, s73, v182\n"
    "v_lshlrev_b32 v198, 16, v183\n"
    "v_and_b32 v199, s73, v183\n"
    "v_sub_f32 v209, 0, v249\n"
    "v_pk_mul_f32 v[192:193], v[192:193], s[58:59]\n"
    "v_pk_mul_f32 v[194:195], v[194:195], s[58:59]\n"
    "v_pk_mul_f32 v[196:197], v[196:197], s[58:59]\n"
    "v_sub_f32 v210, 0, v249\n"
    "v_pk_mul_f32 v[198:199], v[198:199], s[58:59]\n"
    "v_cvt_pk_bf16_f32 v180, v192, v193\n"
    "v_cvt_pk_bf16_f32 v181, v194, v195\n"
    "v_sub_f32 v211, 0, v249\n"
    "v_cvt_pk_bf16_f32 v182, v196, v197\n"
    "v_cvt_pk_bf16_f32 v183, v198, v199\n"
    "v_accvgpr_write_b32 a84, v180\n"
    "v_accvgpr_write_b32 a85, v181\n"
    "v_accvgpr_write_b32 a86, v182\n"
    "v_accvgpr_write_b32 a87, v183\n"
    "v_lshlrev_b32 v192, 16, v184\n"
    "v_and_b32 v193, s73, v184\n"
    "v_lshlrev_b32 v194, 16, v185\n"
    "v_and_b32 v195, s73, v185\n"
    "v_lshlrev_b32 v196, 16, v186\n"
    "v_and_b32 v197, s73, v186\n"
    "v_lshlrev_b32 v198, 16, v187\n"
    "v_and_b32 v199, s73, v187\n"
    "v_pk_mul_f32 v[192:193], v[192:193], s[58:59]\n"
    "v_pk_mul_f32 v[194:195], v[194:195], s[58:59]\n"
    "v_pk_mul_f32 v[196:197], v[196:197], s[58:59]\n"
    "v_pk_mul_f32 v[198:199], v[198:199], s[58:59]\n"
    "v_cvt_pk_bf16_f32 v184, v192, v193\n"
    "v_cvt_pk_bf16_f32 v185, v194, v195\n"
    "v_cvt_pk_bf16_f32 v186, v196, v197\n"
    "v_cvt_pk_bf16_f32 v187, v198, v199\n"
    "v_accvgpr_write_b32 a88, v184\n"
    "v_accvgpr_write_b32 a89, v185\n"
    "v_accvgpr_write_b32 a90, v186\n"
    "v_accvgpr_write_b32 a91, v187\n"
    "v_lshlrev_b32 v192, 16, v188\n"
    "v_and_b32 v193, s73, v188\n"
    "v_lshlrev_b32 v194, 16, v189\n"
    "v_and_b32 v195, s73, v189\n"
    "v_lshlrev_b32 v196, 16, v190\n"
    "v_and_b32 v197, s73, v190\n"
    "v_lshlrev_b32 v198, 16, v191\n"
    "v_and_b32 v199, s73, v191\n"
    "v_pk_mul_f32 v[192:193], v[192:193], s[58:59]\n"
    "v_pk_mul_f32 v[194:195], v[194:195], s[58:59]\n"
    "v_pk_mul_f32 v[196:197], v[196:197], s[58:59]\n"
    "v_pk_mul_f32 v[198:199], v[198:199], s[58:59]\n"
    "v_cvt_pk_bf16_f32 v188, v192, v193\n"
    "v_cvt_pk_bf16_f32 v189, v194, v195\n"
    "v_cvt_pk_bf16_f32 v190, v196, v197\n"
    "v_cvt_pk_bf16_f32 v191, v198, v199\n"
    "v_accvgpr_write_b32 a92, v188\n"
    "v_accvgpr_write_b32 a93, v189\n"
    "v_accvgpr_write_b32 a94, v190\n"
    "v_accvgpr_write_b32 a95, v191\n"
    "s_mov_b32 m0, s46\n"
    "s_nop 0\n"
    "v_mfma_f32_16x16x32_bf16 v[212:215], a[112:115], a[80:83], 0\n"
    "buffer_load_dwordx4 v238, s[24:27], s42 offen offset:0 nt lds\n"
    "s_add_u32 m0, m0, 32\n"
    "v_mfma_f32_16x16x32_bf16 v[212:215], a[116:119], a[84:87], v[212:215]\n"
    "s_nop 0\n"
    "buffer_load_dwordx4 v238, s[24:27], s42 offen offset:1024 nt lds\n"
    "v_mfma_f32_16x16x32_bf16 v[212:215], a[120:123], a[88:91], v[212:215]\n"
    "s_add_u32 m0, m0, 32\n"
    "s_nop 0\n"
    "v_mfma_f32_16x16x32_bf16 v[212:215], a[124:127], a[92:95], v[212:215]\n"
    "buffer_load_dwordx4 v238, s[24:27], s42 offen offset:2048 nt lds\n"
    "s_add_u32 m0, m0, 32\n"
    "s_nop 0\n"
    "buffer_load_dwordx4 v238, s[24:27], s42 offen offset:3072 nt lds\n"
    "s_add_u32 s42, s42, 0x4000\n"
    "s_waitcnt vmcnt(12)\n"
    "ds_read_b128 v[144:147], v244 offset:17408\n"
    "ds_read_b128 v[148:151], v244 offset:17472\n"
    "ds_read_b128 v[152:155], v244 offset:17536\n"
    "ds_read_b128 v[156:159], v244 offset:17600\n"
    "s_waitcnt lgkmcnt(0)\n"
    "v_lshlrev_b32 v192, 16, v144\n"
    "v_and_b32 v193, s73, v144\n"
    "v_lshlrev_b32 v194, 16, v145\n"
    "v_and_b32 v195, s73, v145\n"
    "v_lshlrev_b32 v196, 16, v146\n"
    "v_max3_f32 v251, v212, v213, v214\n"
    "v_and_b32 v197, s73, v146\n"
    "v_lshlrev_b32 v198, 16, v147\n"
    "v_and_b32 v199, s73, v147\n"
    "v_max_f32 v251, v251, v215\n"
    "v_pk_mul_f32 v[192:193], v[192:193], s[58:59]\n"
    "v_pk_mul_f32 v[194:195], v[194:195], s[58:59]\n"
    "v_pk_mul_f32 v[196:197], v[196:197], s[58:59]\n"
    "v_mov_b32 v252, v251\n"
    "v_pk_mul_f32 v[198:199], v[198:199], s[58:59]\n"
    "v_cvt_pk_bf16_f32 v144, v192, v193\n"
    "v_cvt_pk_bf16_f32 v145, v194, v195\n"
    "v_permlane16_swap_b32 v251, v252\n"
    "v_cvt_pk_bf16_f32 v146, v196, v197\n"
    "v_cvt_pk_bf16_f32 v147, v198, v199\n"
    "v_accvgpr_write_b32 a96, v144\n"
    "v_max_f32 v251, v251, v252\n"
    "v_accvgpr_write_b32 a97, v145\n"
    "v_accvgpr_write_b32 a98, v146\n"
    "v_accvgpr_write_b32 a99, v147\n"
    "v_mov_b32 v252, v251\n"
    "v_lshlrev_b32 v192, 16, v148\n"
    "v_and_b32 v193, s73, v148\n"
    "v_lshlrev_b32 v194, 16, v149\n"
    "v_permlane32_swap_b32 v251, v252\n"
    "v_and_b32 v195, s73, v149\n"
    "v_lshlrev_b32 v196, 16, v150\n"
    "v_and_b32 v197, s73, v150\n"
    "v_max_f32 v251, v251, v252\n"
    "v_lshlrev_b32 v198, 16, v151\n"
    "v_and_b32 v199, s73, v151\n"
    "v_pk_mul_f32 v[192:193], v[192:193], s[58:59]\n"
    "v_sub_f32 v212, 0, v251\n"
    "v_pk_mul_f32 v[194:195], v[194:195], s[58:59]\n"
    "v_pk_mul_f32 v[196:197], v[196:197], s[58:59]\n"
    "v_pk_mul_f32 v[198:199], v[198:199], s[58:59]\n"
    "v_sub_f32 v213, 0, v251\n"
    "v_cvt_pk_bf16_f32 v148, v192, v193\n"
    "v_cvt_pk_bf16_f32 v149, v194, v195\n"
    "v_cvt_pk_bf16_f32 v150, v196, v197\n"
    "v_sub_f32 v214, 0, v251\n"
    "v_cvt_pk_bf16_f32 v151, v198, v199\n"
    "v_accvgpr_write_b32 a100, v148\n"
    "v_accvgpr_write_b32 a101, v149\n"
    "v_sub_f32 v215, 0, v251\n"
    "v_accvgpr_write_b32 a102, v150\n"
    "v_accvgpr_write_b32 a103, v151\n"
    "v_lshlrev_b32 v192, 16, v152\n"
    "v_and_b32 v193, s73, v152\n"
    "v_lshlrev_b32 v194, 16, v153\n"
    "v_and_b32 v195, s73, v153\n"
    "v_lshlrev_b32 v196, 16, v154\n"
    "v_and_b32 v197, s73, v154\n"
    "v_lshlrev_b32 v198, 16, v155\n"
    "v_and_b32 v199, s73, v155\n"
    "v_pk_mul_f32 v[192:193], v[192:193], s[58:59]\n"
    "v_pk_mul_f32 v[194:195], v[194:195], s[58:59]\n"
    "v_pk_mul_f32 v[196:197], v[196:197], s[58:59]\n"
    "v_pk_mul_f32 v[198:199], v[198:199], s[58:59]\n"
    "v_cvt_pk_bf16_f32 v152, v192, v193\n"
    "v_cvt_pk_bf16_f32 v153, v194, v195\n"
    "v_cvt_pk_bf16_f32 v154, v196, v197\n"
    "v_cvt_pk_bf16_f32 v155, v198, v199\n"
    "v_accvgpr_write_b32 a104, v152\n"
    "v_accvgpr_write_b32 a105, v153\n"
    "v_accvgpr_write_b32 a106, v154\n"
    "v_accvgpr_write_b32 a107, v155\n"
    "v_lshlrev_b32 v192, 16, v156\n"
    "v_and_b32 v193, s73, v156\n"
    "v_lshlrev_b32 v194, 16, v157\n"
    "v_and_b32 v195, s73, v157\n"
    "v_lshlrev_b32 v196, 16, v158\n"
    "v_and_b32 v197, s73, v158\n"
    "v_lshlrev_b32 v198, 16, v159\n"
    "v_and_b32 v199, s73, v159\n"
    "v_pk_mul_f32 v[192:193], v[192:193], s[58:59]\n"
    "v_pk_mul_f32 v[194:195], v[194:195], s[58:59]\n"
    "v_pk_mul_f32 v[196:197], v[196:197], s[58:59]\n"
    "v_pk_mul_f32 v[198:199], v[198:199], s[58:59]\n"
    "v_cvt_pk_bf16_f32 v156, v192, v193\n"
    "v_cvt_pk_bf16_f32 v157, v194, v195\n"
    "v_cvt_pk_bf16_f32 v158, v196, v197\n"
    "v_cvt_pk_bf16_f32 v159, v198, v199\n"
    "v_accvgpr_write_b32 a108, v156\n"
    "v_accvgpr_write_b32 a109, v157\n"
    "v_accvgpr_write_b32 a110, v158\n"
    "v_accvgpr_write_b32 a111, v159\n"
    "s_mov_b32 s38, 0\n"
    "s_mov_b32 m0, s56\n"
    "s_nop 0\n"
    "buffer_load_dwordx4 v237, s[36:39], s54 offen offset:0 lds\n"
    "buffer_load_dwordx4 v237, s[36:39], s54 offen offset:0 lds\n"
    "buffer_load_dwordx4 v237, s[36:39], s54 offen offset:0 lds\n"
    "buffer_load_dwordx4 v237, s[36:39], s54 offen offset:0 lds\n"
    "s_mov_b32 s44, s54\n"
    "s_add_u32 s55, s81, s71\n"
    "s_add_u32 s64, s52, 1\n"
    "s_mul_i32 s62, s64, 77824\n"
    "s_add_u32 s36, s88, s62\n"
    "s_addc_u32 s37, s89, 0\n"
    "s_mov_b32 s57, 77824\n"
    "s_mov_b32 s60, 0\n"
    "s_add_u32 s49, s81, s71\n"
    "s_mov_b32 s50, 2176\n"
    "s_nop 1\n"
    "v_mfma_f32_16x16x32_bf16 v[216:219], a[112:115], a[96:99], 0\n"
    "v_mfma_f32_16x16x32_bf16 v[216:219], a[116:119], a[100:103], v[216:219]\n"
    "v_mfma_f32_16x16x32_bf16 v[216:219], a[120:123], a[104:107], v[216:219]\n"
    "v_mfma_f32_16x16x32_bf16 v[216:219], a[124:127], a[108:111], v[216:219]\n"
    "s_nop 7\n"
    "v_mov_b32 v192, 0\n"
    "v_mov_b32 v193, 0\n"
    "v_max3_f32 v245, v216, v217, v218\n"
    "v_mov_b32 v194, 0\n"
    "v_mov_b32 v195, 0\n"
    "v_max_f32 v245, v245, v219\n"
    "v_mov_b32 v196, 0\n"
    "v_mov_b32 v197, 0\n"
    "v_mov_b32 v246, v245\n"
    "v_mov_b32 v198, 0\n"
    "v_mov_b32 v199, 0\n"
    "v_permlane16_swap_b32 v245, v246\n"
    "v_mov_b32 v220, 0\n"
    "v_mov_b32 v228, 0\n"
    "v_max_f32 v245, v245, v246\n"
    "v_mov_b32 v229, 0\n"
    "v_mov_b32 v230, 0\n"
    "v_mov_b32 v246, v245\n"
    "v_mov_b32 v231, 0\n"
    "v_mov_b32 v232, 0\n"
    "v_permlane32_swap_b32 v245, v246\n"
    "s_nop 0\n"
    "s_nop 0\n"
    "v_max_f32 v245, v245, v246\n"
    "s_nop 0\n"
    "s_nop 0\n"
    "v_add_f32 v245, s63, v245\n"
    "s_nop 0\n"
    "s_nop 0\n"
    "v_sub_f32 v216, 0, v245\n"
    "s_nop 0\n"
    "s_nop 0\n"
    "v_sub_f32 v217, 0, v245\n"
    "s_nop 0\n"
    "s_nop 0\n"
    "v_sub_f32 v218, 0, v245\n"
    "s_nop 0\n"
    "s_nop 0\n"
    "v_sub_f32 v219, 0, v245\n"
    "v_exp_f32 v224, v128\n"
    "v_exp_f32 v225, v129\n"
    "v_exp_f32 v226, v130\n"
    "s_mov_b32 s51, 0\n"
    "v_cvt_pk_bf16_f32 v176, v224, v225\n"
    "s_waitcnt vmcnt(12) lgkmcnt(0)\n"
    "s_barrier\n"
    "HEAD_%=:\n"
    "s_cmp_eq_u32 s40, 3\n"
    "s_cbranch_scc1 LOOPW_%=\n"
    ".p2align 6\n"
    "LOOP_%=:\n"
    "v_mfma_f32_16x16x32_bf16 v[144:147], a[112:115], a[48:51], v[204:207]\n"
    "s_cmp_lt_u32 s42, s43\n"
    "v_exp_f32 v227, v131\n"
    "v_mfma_f32_16x16x32_bf16 v[144:147], a[116:119], a[52:55], v[144:147]\n"
    "s_cselect_b32 s22, 134217728, 0\n"
    "s_add_u32 s45, s47, s41\n"
    "v_cvt_pk_bf16_f32 v177, v226, v227\n"
    "v_mfma_f32_16x16x32_bf16 v[144:147], a[120:123], a[56:59], v[144:147]\n"
    "s_mov_b32 m0, s45\n"
    "v_exp_f32 v224, v132\n"
    "v_mfma_f32_16x16x32_bf16 v[144:147], a[124:127], a[60:63], v[144:147]\n"
    "buffer_load_dwordx4 v237, s[20:23], s42 offen offset:0 nt lds\n"
    "v_mfma_f32_16x16x32_bf16 v[148:151], a[128:131], a[48:51], v[204:207]\n"
    "s_mov_b32 s26, s22\n"
    "v_exp_f32 v225, v133\n"
    "v_mfma_f32_16x16x32_bf16 v[148:151], a[132:135], a[52:55], v[148:151]\n"
    "s_add_u32 s46, s45, 17408\n"
    "v_cvt_pk_bf16_f32 v178, v224, v225\n"
    "v_add_f32 v232, v232, v220\n"
    "v_mfma_f32_16x16x32_bf16 v[148:151], a[136:139], a[56:59], v[148:151]\n"
    "s_add_u32 m0, m0, 64\n"
    "v_exp_f32 v226, v134\n"
    "v_mfma_f32_16x16x32_bf16 v[148:151], a[140:143], a[60:63], v[148:151]\n"
    "buffer_load_dwordx4 v237, s[20:23], s42 offen offset:1024 nt lds\n"
    "v_mfma_f32_16x16x32_bf16 v[152:155], a[144:147], a[48:51], v[204:207]\n"
    "v_exp_f32 v227, v135\n"
    "v_mfma_f32_16x16x32_bf16 v[152:155], a[148:151], a[52:55], v[152:155]\n"
    "v_add_u32 v234, s47, v236\n"
    "v_cvt_pk_bf16_f32 v179, v226, v227\n"
    "v_mfma_f32_16x16x32_bf16 v[152:155], a[152:155], a[56:59], v[152:155]\n"
    "s_add_u32 m0, m0, 64\n"
    "v_exp_f32 v224, v136\n"
    "v_mfma_f32_16x16x32_bf16 v[152:155], a[156:159], a[60:63], v[152:155]\n"
    "buffer_load_dwordx4 v237, s[20:23], s42 offen offset:2048 nt lds\n"
    "v_mfma_f32_16x16x32_bf16 v[156:159], a[160:163], a[48:51], v[204:207]\n"
    "v_exp_f32 v225, v137\n"
    "v_mfma_f32_16x16x32_bf16 v[156:159], a[164:167], a[52:55], v[156:159]\n"
    "v_cvt_pk_bf16_f32 v180, v224, v225\n"
    "v_mfma_f32_16x16x32_bf16 v[156:159], a[168:171], a[56:59], v[156:159]\n"
    "s_add_u32 m0, m0, 64\n"
    "v_exp_f32 v226, v138\n"
    "v_mfma_f32_16x16x32_bf16 v[156:159], a[172:175], a[60:63], v[156:159]\n"
    "buffer_load_dwordx4 v237, s[20:23], s42 offen offset:3072 nt lds\n"
    "v_mfma_f32_16x16x32_bf16 a[0:3], a[176:179], v[192:195], a[0:3]\n"
    "v_exp_f32 v227, v139\n"
    "v_mfma_f32_16x16x32_bf16 a[0:3], a[180:183], v[196:199], a[0:3]\n"
    "ds_read_b64_tr_b16 a[176:177], v234 offset:0\n"
    "v_cvt_pk_bf16_f32 v181, v226, v227\n"
    "v_mfma_f32_16x16x32_bf16 a[4:7], a[184:187], v[192:195], a[4:7]\n"
    "v_exp_f32 v224, v140\n"
    "v_mfma_f32_16x16x32_bf16 a[4:7], a[188:191], v[196:199], a[4:7]\n"
    "ds_read_b64_tr_b16 a[178:179], v234 offset:4352\n"
    "ds_read_b64_tr_b16 a[180:181], v234 offset:8704\n"
    "v_mfma_f32_16x16x32_bf16 a[8:11], a[192:195], v[192:195], a[8:11]\n"
    "v_exp_f32 v225, v141\n"
    "v_mfma_f32_16x16x32_bf16 a[8:11], a[196:199], v[196:199], a[8:11]\n"
    "ds_read_b64_tr_b16 a[182:183], v234 offset:13056\n"
    "v_cvt_pk_bf16_f32 v182, v224, v225\n"
    "v_mfma_f32_16x16x32_bf16 a[12:15], a[200:203], v[192:195], a[12:15]\n"
    "v_exp_f32 v226, v142\n"
    "v_mfma_f32_16x16x32_bf16 a[12:15], a[204:207], v[196:199], a[12:15]\n"
    "ds_read_b64_tr_b16 a[184:185], v234 offset:32\n"
    "ds_read_b64_tr_b16 a[186:187], v234 offset:4384\n"
    "v_mfma_f32_16x16x32_bf16 a[16:19], a[208:211], v[192:195], a[16:19]\n"
    "v_exp_f32 v227, v143\n"
    "v_mfma_f32_16x16x32_bf16 a[16:19], a[212:215], v[196:199], a[16:19]\n"
    "ds_read_b64_tr_b16 a[188:189], v234 offset:8736\n"
    "v_cvt_pk_bf16_f32 v183, v226, v227\n"
    "v_mfma_f32_16x16x32_bf16 v[220:223], a[240:243], v[176:179], 0\n"
    "ds_read_b64_tr_b16 a[190:191], v234 offset:13088\n"
    "ds_read_b64_tr_b16 a[192:193], v234 offset:256\n"
    "v_mfma_f32_16x16x32_bf16 v[220:223], a[240:243], v[180:183], v[220:223]\n"
    "ds_read_b64_tr_b16 a[194:195], v234 offset:4608\n"
    "ds_read_b64_tr_b16 a[196:197], v234 offset:8960\n"
    "v_mfma_f32_16x16x32_bf16 a[20:23], a[216:219], v[192:195], a[20:23]\n"
    "v_exp_f32 v224, v144\n"
    "v_mfma_f32_16x16x32_bf16 a[20:23], a[220:223], v[196:199], a[20:23]\n"
    "ds_read_b64_tr_b16 a[198:199], v234 offset:13312\n"
    "ds_read_b64_tr_b16 a[200:201], v234 offset:288\n"
    "v_mfma_f32_16x16x32_bf16 a[24:27], a[224:227], v[192:195], a[24:27]\n"
    "v_exp_f32 v225, v145\n"
    "v_mfma_f32_16x16x32_bf16 a[24:27], a[228:231], v[196:199], a[24:27]\n"
    "ds_read_b64_tr_b16 a[202:203], v234 offset:4640\n"
    "v_cmp_nge_f32 vcc, s53, v220\n"
    "v_mfma_f32_16x16x32_bf16 a[28:31], a[232:235], v[192:195], a[28:31]\n"
    "v_exp_f32 v226, v146\n"
    "v_mfma_f32_16x16x32_bf16 a[28:31], a[236:239], v[196:199], a[28:31]\n"
    "s_cbranch_vccnz RARE0_%=\n"
    "RET0_%=:\n"
    "ds_read_b64_tr_b16 a[204:205], v234 offset:8992\n"
    "v_cvt_pk_bf16_f32 v184, v224, v225\n"
    "v_mfma_f32_16x16x32_bf16 v[128:131], a[112:115], a[64:67], v[208:211]\n"
    "v_exp_f32 v227, v147\n"
    "v_mfma_f32_16x16x32_bf16 v[128:131], a[116:119], a[68:71], v[128:131]\n"
    "ds_read_b64_tr_b16 a[206:207], v234 offset:13344\n"
    "v_cvt_pk_bf16_f32 v185, v226, v227\n"
    "v_mfma_f32_16x16x32_bf16 v[128:131], a[120:123], a[72:75], v[128:131]\n"
    "v_exp_f32 v224, v148\n"
    "v_mfma_f32_16x16x32_bf16 v[128:131], a[124:127], a[76:79], v[128:131]\n"
    "ds_read_b64_tr_b16 a[208:209], v234 offset:512\n"
    "v_add_f32 v228, v228, v220\n"
    "v_mfma_f32_16x16x32_bf16 v[132:135], a[128:131], a[64:67], v[208:211]\n"
    "v_exp_f32 v225, v149\n"
    "v_mfma_f32_16x16x32_bf16 v[132:135], a[132:135], a[68:71], v[132:135]\n"
    "ds_read_b64_tr_b16 a[210:211], v234 offset:4864\n"
    "v_cvt_pk_bf16_f32 v186, v224, v225\n"
    "v_mfma_f32_16x16x32_bf16 v[132:135], a[136:139], a[72:75], v[132:135]\n"
    "v_exp_f32 v226, v150\n"
    "v_mfma_f32_16x16x32_bf16 v[132:135], a[140:143], a[76:79], v[132:135]\n"
    "ds_read_b64_tr_b16 a[212:213], v234 offset:9216\n"
    "ds_read_b64_tr_b16 a[214:215], v234 offset:13568\n"
    "v_mfma_f32_16x16x32_bf16 v[136:139], a[144:147], a[64:67], v[208:211]\n"
    "v_exp_f32 v227, v151\n"
    "v_mfma_f32_16x16x32_bf16 v[136:139], a[148:151], a[68:71], v[136:139]\n"
    "ds_read_b64_tr_b16 a[216:217], v234 offset:544\n"
    "v_cvt_pk_bf16_f32 v187, v226, v227\n"
    "v_mfma_f32_16x16x32_bf16 v[136:139], a[152:155], a[72:75], v[136:139]\n"
    "v_exp_f32 v224, v152\n"
    "v_mfma_f32_16x16x32_bf16 v[136:139], a[156:159], a[76:79], v[136:139]\n"
    "ds_read_b64_tr_b16 a[218:219], v234 offset:4896\n"
    "ds_read_b64_tr_b16 a[220:221], v234 offset:9248\n"
    "v_mfma_f32_16x16x32_bf16 v[140:143], a[160:163], a[64:67], v[208:211]\n"
    "v_exp_f32 v225, v153\n"
    "v_mfma_f32_16x16x32_bf16 v[140:143], a[164:167], a[68:71], v[140:143]\n"
    "ds_read_b64_tr_b16 a[222:223], v234 offset:13600\n"
    "v_cvt_pk_bf16_f32 v188, v224, v225\n"
    "v_mfma_f32_16x16x32_bf16 v[140:143], a[168:171], a[72:75], v[140:143]\n"
    "v_exp_f32 v226, v154\n"
    "v_mfma_f32_16x16x32_bf16 v[140:143], a[172:175], a[76:79], v[140:143]\n"
    "s_waitcnt lgkmcnt(15)\n"
    "ds_read_b64_tr_b16 a[224:225], v234 offset:768\n"
    "ds_read_b64_tr_b16 a[226:227], v234 offset:5120\n"
    "v_mfma_f32_16x16x32_bf16 v[0:3], a[176:179], v[176:179], v[0:3]\n"
    "v_exp_f32 v227, v155\n"
    "v_mfma_f32_16x16x32_bf16 v[0:3], a[180:183], v[180:183], v[0:3]\n"
    "ds_read_b64_tr_b16 a[228:229], v234 offset:9472\n"
    "v_cvt_pk_bf16_f32 v189, v226, v227\n"
    "v_mfma_f32_16x16x32_bf16 v[4:7], a[184:187], v[176:179], v[4:7]\n"
    "v_exp_f32 v224, v156\n"
    "v_mfma_f32_16x16x32_bf16 v[4:7], a[188:191], v[180:183], v[4:7]\n"
    "s_waitcnt lgkmcnt(11)\n"
    "ds_read_b64_tr_b16 a[230:231], v234 offset:13824\n"
    "ds_read_b64_tr_b16 a[232:233], v234 offset:800\n"
    "v_mfma_f32_16x16x32_bf16 v[8:11], a[192:195], v[176:179], v[8:11]\n"
    "v_exp_f32 v225, v157\n"
    "v_mfma_f32_16x16x32_bf16 v[8:11], a[196:199], v[180:183], v[8:11]\n"
    "ds_read_b64_tr_b16 a[234:235], v234 offset:5152\n"
    "v_cvt_pk_bf16_f32 v190, v224, v225\n"
    "v_mfma_f32_16x16x32_bf16 v[12:15], a[200:203], v[176:179], v[12:15]\n"
    "v_exp_f32 v226, v158\n"
    "v_mfma_f32_16x16x32_bf16 v[12:15], a[204:207], v[180:183], v[12:15]\n"
    "s_waitcnt lgkmcnt(10)\n"
    "ds_read_b64_tr_b16 a[236:237], v234 offset:9504\n"
    "ds_read_b64_tr_b16 a[238:239], v234 offset:13856\n"
    "v_mfma_f32_16x16x32_bf16 v[16:19], a[208:211], v[176:179], v[16:19]\n"
    "v_exp_f32 v227, v159\n"
    "v_mfma_f32_16x16x32_bf16 v[16:19], a[212:215], v[180:183], v[16:19]\n"
    "v_cvt_pk_bf16_f32 v191, v226, v227\n"
    "v_mfma_f32_16x16x32_bf16 v[220:223], a[240:243], v[184:187], 0\n"
    "s_nop 0\n"
    "v_mfma_f32_16x16x32_bf16 v[220:223], a[240:243], v[188:191], v[220:223]\n"
    "s_waitcnt lgkmcnt(8)\n"
    "v_add_u32 v253, 1, v253\n"
    "v_mfma_f32_16x16x32_bf16 v[20:23], a[216:219], v[176:179], v[20:23]\n"
    "v_exp_f32 v224, v128\n"
    "v_mfma_f32_16x16x32_bf16 v[20:23], a[220:223], v[180:183], v[20:23]\n"
    "s_waitcnt lgkmcnt(4)\n"
    "v_mfma_f32_16x16x32_bf16 v[24:27], a[224:227], v[176:179], v[24:27]\n"
    "v_exp_f32 v225, v129\n"
    "v_mfma_f32_16x16x32_bf16 v[24:27], a[228:231], v[180:183], v[24:27]\n"
    "s_waitcnt lgkmcnt(0)\n"
    "v_cmp_nge_f32 vcc, s53, v220\n"
    "v_mfma_f32_16x16x32_bf16 v[28:31], a[232:235], v[176:179], v[28:31]\n"
    "v_exp_f32 v226, v130\n"
    "v_mfma_f32_16x16x32_bf16 v[28:31], a[236:239], v[180:183], v[28:31]\n"
    "s_cbranch_vccnz RARE1_%=\n"
    "RET1_%=:\n"
    "v_cvt_pk_bf16_f32 v176, v224, v225\n"
    "v_mfma_f32_16x16x32_bf16 v[144:147], a[112:115], a[80:83], v[212:215]\n"
    "s_waitcnt vmcnt(12) lgkmcnt(0)\n"
    "v_exp_f32 v227, v131\n"
    "v_mfma_f32_16x16x32_bf16 v[144:147], a[116:119], a[84:87], v[144:147]\n"
    "ds_write_b32 v254, v253 offset:0\n"
    "v_cvt_pk_bf16_f32 v177, v226, v227\n"
    "v_mfma_f32_16x16x32_bf16 v[144:147], a[120:123], a[88:91], v[144:147]\n"
    "v_exp_f32 v224, v132\n"
    "v_mfma_f32_16x16x32_bf16 v[144:147], a[124:127], a[92:95], v[144:147]\n"
    "v_add_f32 v229, v229, v220\n"
    "v_mfma_f32_16x16x32_bf16 v[148:151], a[128:131], a[80:83], v[212:215]\n"
    "v_exp_f32 v225, v133\n"
    "v_mfma_f32_16x16x32_bf16 v[148:151], a[132:135], a[84:87], v[148:151]\n"
    "v_cvt_pk_bf16_f32 v178, v224, v225\n"
    "v_mfma_f32_16x16x32_bf16 v[148:151], a[136:139], a[88:91], v[148:151]\n"
    "v_exp_f32 v226, v134\n"
    "v_mfma_f32_16x16x32_bf16 v[148:151], a[140:143], a[92:95], v[148:151]\n"
    "v_mfma_f32_16x16x32_bf16 v[152:155], a[144:147], a[80:83], v[212:215]\n"
    "v_exp_f32 v227, v135\n"
    "v_mfma_f32_16x16x32_bf16 v[152:155], a[148:151], a[84:87], v[152:155]\n"
    "ds_read_b32 v252, v255 offset:0\n"
    "v_cvt_pk_bf16_f32 v179, v226, v227\n"
    "v_mfma_f32_16x16x32_bf16 v[152:155], a[152:155], a[88:91], v[152:155]\n"
    "v_exp_f32 v224, v136\n"
    "v_mfma_f32_16x16x32_bf16 v[152:155], a[156:159], a[92:95], v[152:155]\n"
    "v_mfma_f32_16x16x32_bf16 v[156:159], a[160:163], a[80:83], v[212:215]\n"
    "v_exp_f32 v225, v137\n"
    "v_mfma_f32_16x16x32_bf16 v[156:159], a[164:167], a[84:87], v[156:159]\n"
    "v_cvt_pk_bf16_f32 v180, v224, v225\n"
    "v_mfma_f32_16x16x32_bf16 v[156:159], a[168:171], a[88:91], v[156:159]\n"
    "v_exp_f32 v226, v138\n"
    "v_mfma_f32_16x16x32_bf16 v[156:159], a[172:175], a[92:95], v[156:159]\n"
    "s_waitcnt lgkmcnt(0)\n"
    "v_cmp_gt_u32 vcc, v253, v252\n"
    "v_mfma_f32_16x16x32_bf16 v[32:35], a[176:179], v[184:187], v[32:35]\n"
    "v_exp_f32 v227, v139\n"
    "v_mfma_f32_16x16x32_bf16 v[32:35], a[180:183], v[188:191], v[32:35]\n"
    "s_cbranch_vccnz SPIN1_%=\n"
    "SPIN1_RET_%=:\n"
    "v_cvt_pk_bf16_f32 v181, v226, v227\n"
    "v_mfma_f32_16x16x32_bf16 v[36:39], a[184:187], v[184:187], v[36:39]\n"
    "s_mov_b32 m0, s46\n"
    "v_exp_f32 v224, v140\n"
    "v_mfma_f32_16x16x32_bf16 v[36:39], a[188:191], v[188:191], v[36:39]\n"
    "buffer_load_dwordx4 v238, s[24:27], s42 offen offset:0 nt lds\n"
    "v_mfma_f32_16x16x32_bf16 v[40:43], a[192:195], v[184:187], v[40:43]\n"
    "v_exp_f32 v225, v141\n"
    "v_mfma_f32_16x16x32_bf16 v[40:43], a[196:199], v[188:191], v[40:43]\n"
    "v_cvt_pk_bf16_f32 v182, v224, v225\n"
    "v_mfma_f32_16x16x32_bf16 v[44:47], a[200:203], v[184:187], v[44:47]\n"
    "s_add_u32 m0, m0, 32\n"
    "v_exp_f32 v226, v142\n"
    "v_mfma_f32_16x16x32_bf16 v[44:47], a[204:207], v[188:191], v[44:47]\n"
    "buffer_load_dwordx4 v238, s[24:27], s42 offen offset:1024 nt lds\n"
    "v_mfma_f32_16x16x32_bf16 v[48:51], a[208:211], v[184:187], v[48:51]\n"
    "v_exp_f32 v227, v143\n"
    "v_mfma_f32_16x16x32_bf16 v[48:51], a[212:215], v[188:191], v[48:51]\n"
    "v_cvt_pk_bf16_f32 v183, v226, v227\n"
    "v_mfma_f32_16x16x32_bf16 v[220:223], a[240:243], v[176:179], 0\n"
    "s_add_u32 m0, m0, 32\n"
    "v_mfma_f32_16x16x32_bf16 v[220:223], a[240:243], v[180:183], v[220:223]\n"
    "buffer_load_dwordx4 v238, s[24:27], s42 offen offset:2048 nt lds\n"
    "v_mfma_f32_16x16x32_bf16 v[52:55], a[216:219], v[184:187], v[52:55]\n"
    "s_add_u32 m0, m0, 32\n"
    "v_exp_f32 v224, v144\n"
    "v_mfma_f32_16x16x32_bf16 v[52:55], a[220:223], v[188:191], v[52:55]\n"
    "buffer_load_dwordx4 v238, s[24:27], s42 offen offset:3072 nt lds\n"
    "v_mfma_f32_16x16x32_bf16 v[56:59], a[224:227], v[184:187], v[56:59]\n"
    "v_exp_f32 v225, v145\n"
    "v_mfma_f32_16x16x32_bf16 v[56:59], a[228:231], v[188:191], v[56:59]\n"
    "v_cmp_nge_f32 vcc, s53, v220\n"
    "v_mfma_f32_16x16x32_bf16 v[60:63], a[232:235], v[184:187], v[60:63]\n"
    "s_add_u32 s42, s42, 0x4000\n"
    "v_exp_f32 v226, v146\n"
    "v_mfma_f32_16x16x32_bf16 v[60:63], a[236:239], v[188:191], v[60:63]\n"
    "s_cbranch_vccnz RARE2_%=\n"
    "RET2_%=:\n"
    "v_add_u32 v233, s48, v241\n"
    "v_cvt_pk_bf16_f32 v184, v224, v225\n"
    "v_mfma_f32_16x16x32_bf16 v[160:163], a[112:115], a[96:99], v[216:219]\n"
    "v_exp_f32 v227, v147\n"
    "v_mfma_f32_16x16x32_bf16 v[160:163], a[116:119], a[100:103], v[160:163]\n"
    "ds_read_b128 a[112:115], v233 offset:0\n"
    "v_cvt_pk_bf16_f32 v185, v226, v227\n"
    "v_mfma_f32_16x16x32_bf16 v[160:163], a[120:123], a[104:107], v[160:163]\n"
    "v_exp_f32 v224, v148\n"
    "v_mfma_f32_16x16x32_bf16 v[160:163], a[124:127], a[108:111], v[160:163]\n"
    "ds_read_b128 a[116:119], v233 offset:64\n"
    "v_add_f32 v230, v230, v220\n"
    "v_mfma_f32_16x16x32_bf16 v[164:167], a[128:131], a[96:99], v[216:219]\n"
    "v_exp_f32 v225, v149\n"
    "v_mfma_f32_16x16x32_bf16 v[164:167], a[132:135], a[100:103], v[164:167]\n"
    "ds_read_b128 a[120:123], v233 offset:128\n"
    "v_cvt_pk_bf16_f32 v186, v224, v225\n"
    "v_mfma_f32_16x16x32_bf16 v[164:167], a[136:139], a[104:107], v[164:167]\n"
    "v_exp_f32 v226, v150\n"
    "v_mfma_f32_16x16x32_bf16 v[164:167], a[140:143], a[108:111], v[164:167]\n"
    "ds_read_b128 a[124:127], v233 offset:192\n"
    "ds_read_b128 a[128:131], v233 offset:4352\n"
    "v_mfma_f32_16x16x32_bf16 v[168:171], a[144:147], a[96:99], v[216:219]\n"
    "v_exp_f32 v227, v151\n"
    "v_mfma_f32_16x16x32_bf16 v[168:171], a[148:151], a[100:103], v[168:171]\n"
    "ds_read_b128 a[132:135], v233 offset:4416\n"
    "v_cvt_pk_bf16_f32 v187, v226, v227\n"
    "v_mfma_f32_16x16x32_bf16 v[168:171], a[152:155], a[104:107], v[168:171]\n"
    "v_exp_f32 v224, v152\n"
    "v_mfma_f32_16x16x32_bf16 v[168:171], a[156:159], a[108:111], v[168:171]\n"
    "ds_read_b128 a[136:139], v233 offset:4480\n"
    "ds_read_b128 a[140:143], v233 offset:4544\n"
    "v_mfma_f32_16x16x32_bf16 v[172:175], a[160:163], a[96:99], v[216:219]\n"
    "v_exp_f32 v225, v153\n"
    "v_mfma_f32_16x16x32_bf16 v[172:175], a[164:167], a[100:103], v[172:175]\n"
    "ds_read_b128 a[144:147], v233 offset:8704\n"
    "v_cvt_pk_bf16_f32 v188, v224, v225\n"
    "v_mfma_f32_16x16x32_bf16 v[172:175], a[168:171], a[104:107], v[172:175]\n"
    "v_exp_f32 v226, v154\n"
    "v_mfma_f32_16x16x32_bf16 v[172:175], a[172:175], a[108:111], v[172:175]\n"
    "ds_read_b128 a[148:151], v233 offset:8768\n"
    "ds_read_b128 a[152:155], v233 offset:8832\n"
    "v_mfma_f32_16x16x32_bf16 v[64:67], a[176:179], v[176:179], v[64:67]\n"
    "v_exp_f32 v227, v155\n"
    "v_mfma_f32_16x16x32_bf16 v[64:67], a[180:183], v[180:183], v[64:67]\n"
    "ds_read_b128 a[156:159], v233 offset:8896\n"
    "v_cvt_pk_bf16_f32 v189, v226, v227\n"
    "v_mfma_f32_16x16x32_bf16 v[68:71], a[184:187], v[176:179], v[68:71]\n"
    "v_exp_f32 v224, v156\n"
    "v_mfma_f32_16x16x32_bf16 v[68:71], a[188:191], v[180:183], v[68:71]\n"
    "ds_read_b128 a[160:163], v233 offset:13056\n"
    "ds_read_b128 a[164:167], v233 offset:13120\n"
    "v_mfma_f32_16x16x32_bf16 v[72:75], a[192:195], v[176:179], v[72:75]\n"
    "v_exp_f32 v225, v157\n"
    "v_mfma_f32_16x16x32_bf16 v[72:75], a[196:199], v[180:183], v[72:75]\n"
    "ds_read_b128 a[168:171], v233 offset:13184\n"
    "v_cvt_pk_bf16_f32 v190, v224, v225\n"
    "v_mfma_f32_16x16x32_bf16 v[76:79], a[200:203], v[176:179], v[76:79]\n"
    "v_exp_f32 v226, v158\n"
    "v_mfma_f32_16x16x32_bf16 v[76:79], a[204:207], v[180:183], v[76:79]\n"
    "ds_read_b128 a[172:175], v233 offset:13248\n"
    "v_mfma_f32_16x16x32_bf16 v[80:83], a[208:211], v[176:179], v[80:83]\n"
    "v_exp_f32 v227, v159\n"
    "v_mfma_f32_16x16x32_bf16 v[80:83], a[212:215], v[180:183], v[80:83]\n"
    "v_cvt_pk_bf16_f32 v191, v226, v227\n"
    "v_mfma_f32_16x16x32_bf16 v[220:223], a[240:243], v[184:187], 0\n"
    "s_nop 0\n"
    "v_mfma_f32_16x16x32_bf16 v[220:223], a[240:243], v[188:191], v[220:223]\n"
    "v_mfma_f32_16x16x32_bf16 v[84:87], a[216:219], v[176:179], v[84:87]\n"
    "v_exp_f32 v224, v160\n"
    "v_mfma_f32_16x16x32_bf16 v[84:87], a[220:223], v[180:183], v[84:87]\n"
    "s_nop 0\n"
    "v_add_u32 v253, 1, v253\n"
    "v_mfma_f32_16x16x32_bf16 v[88:91], a[224:227], v[176:179], v[88:91]\n"
    "v_exp_f32 v225, v161\n"
    "v_mfma_f32_16x16x32_bf16 v[88:91], a[228:231], v[180:183], v[88:91]\n"
    "v_cmp_nge_f32 vcc, s53, v220\n"
    "v_mfma_f32_16x16x32_bf16 v[92:95], a[232:235], v[176:179], v[92:95]\n"
    "v_exp_f32 v226, v162\n"
    "v_mfma_f32_16x16x32_bf16 v[92:95], a[236:239], v[180:183], v[92:95]\n"
    "s_waitcnt lgkmcnt(0)\n"
    "s_cbranch_vccnz RARE3_%=\n"
    "RET3_%=:\n"
    "v_cvt_pk_bf16_f32 v192, v224, v225\n"
    "v_mfma_f32_16x16x32_bf16 v[128:131], a[112:115], a[32:35], v[200:203]\n"
    "s_waitcnt vmcnt(12)\n"
    "v_exp_f32 v227, v163\n"
    "v_mfma_f32_16x16x32_bf16 v[128:131], a[116:119], a[36:39], v[128:131]\n"
    "s_bitcmp1_b32 s61, s51\n"
    "ds_write_b32 v254, v253 offset:0\n"
    "v_cvt_pk_bf16_f32 v193, v226, v227\n"
    "v_mfma_f32_16x16x32_bf16 v[128:131], a[120:123], a[40:43], v[128:131]\n"
    "s_cselect_b32 s38, s57, 0\n"
    "v_exp_f32 v224, v164\n"
    "v_mfma_f32_16x16x32_bf16 v[128:131], a[124:127], a[44:47], v[128:131]\n"
    "s_cselect_b32 s30, s60, 0\n"
    "v_add_u32 v252, s55, v239\n"
    "v_mfma_f32_16x16x32_bf16 v[132:135], a[128:131], a[32:35], v[200:203]\n"
    "s_cselect_b32 m0, s49, s56\n"
    "v_exp_f32 v225, v165\n"
    "v_mfma_f32_16x16x32_bf16 v[132:135], a[132:135], a[36:39], v[132:135]\n"
    "s_cselect_b32 s68, 2048, 0\n"
    "v_cvt_pk_bf16_f32 v194, v224, v225\n"
    "v_add_f32 v231, v231, v220\n"
    "v_mfma_f32_16x16x32_bf16 v[132:135], a[136:139], a[40:43], v[132:135]\n"
    "s_cselect_b32 s67, s50, 0\n"
    "v_exp_f32 v226, v166\n"
    "v_mfma_f32_16x16x32_bf16 v[132:135], a[140:143], a[44:47], v[132:135]\n"
    "s_cselect_b32 s66, 2176, 0\n"
    "ds_read_b128 v[244:247], v252 offset:0\n"
    "ds_read_b128 v[248:251], v252 offset:1088\n"
    "v_mfma_f32_16x16x32_bf16 v[136:139], a[144:147], a[32:35], v[200:203]\n"
    "v_exp_f32 v227, v167\n"
    "v_mfma_f32_16x16x32_bf16 v[136:139], a[148:151], a[36:39], v[136:139]\n"
    "v_cvt_pk_bf16_f32 v195, v226, v227\n"
    "v_mfma_f32_16x16x32_bf16 v[136:139], a[152:155], a[40:43], v[136:139]\n"
    "v_exp_f32 v224, v168\n"
    "v_mfma_f32_16x16x32_bf16 v[136:139], a[156:159], a[44:47], v[136:139]\n"
    "buffer_load_dwordx4 v237, s[36:39], s44 offen offset:0 nt lds\n"
    "v_mfma_f32_16x16x32_bf16 v[140:143], a[160:163], a[32:35], v[200:203]\n"
    "s_add_u32 m0, m0, 64\n"
    "v_exp_f32 v225, v169\n"
    "v_mfma_f32_16x16x32_bf16 v[140:143], a[164:167], a[36:39], v[140:143]\n"
    "ds_read_b32 v252, v255 offset:0\n"
    "v_cvt_pk_bf16_f32 v196, v224, v225\n"
    "v_mfma_f32_16x16x32_bf16 v[140:143], a[168:171], a[40:43], v[140:143]\n"
    "v_exp_f32 v226, v170\n"
    "v_mfma_f32_16x16x32_bf16 v[140:143], a[172:175], a[44:47], v[140:143]\n"
    "buffer_load_dwordx4 v237, s[36:39], s44 offen offset:1024 nt lds\n"
    "v_mfma_f32_16x16x32_bf16 v[96:99], a[176:179], v[184:187], v[96:99]\n"
    "s_waitcnt lgkmcnt(1)\n"
    "v_exp_f32 v227, v171\n"
    "v_mfma_f32_16x16x32_bf16 v[96:99], a[180:183], v[188:191], v[96:99]\n"
    "v_cvt_pk_bf16_f32 v197, v226, v227\n"
    "v_mfma_f32_16x16x32_bf16 v[100:103], a[184:187], v[184:187], v[100:103]\n"
    "v_exp_f32 v224, v172\n"
    "v_mfma_f32_16x16x32_bf16 v[100:103], a[188:191], v[188:191], v[100:103]\n"
    "buffer_store_dwordx4 v[244:247], v240, s[28:31], s44 offen offset:0 nt\n"
    "v_mfma_f32_16x16x32_bf16 v[104:107], a[192:195], v[184:187], v[104:107]\n"
    "v_exp_f32 v225, v173\n"
    "v_mfma_f32_16x16x32_bf16 v[104:107], a[196:199], v[188:191], v[104:107]\n"
    "s_waitcnt lgkmcnt(0)\n"
    "v_cmp_gt_u32 vcc, v253, v252\n"
    "v_cvt_pk_bf16_f32 v198, v224, v225\n"
    "v_mfma_f32_16x16x32_bf16 v[108:111], a[200:203], v[184:187], v[108:111]\n"
    "v_exp_f32 v226, v174\n"
    "v_mfma_f32_16x16x32_bf16 v[108:111], a[204:207], v[188:191], v[108:111]\n"
    "s_cbranch_vccnz SPIN2_%=\n"
    "SPIN2_RET_%=:\n"
    "buffer_store_dwordx4 v[248:251], v240, s[28:31], s44 offen offset:1024 nt\n"
    "v_mfma_f32_16x16x32_bf16 v[112:115], a[208:211], v[184:187], v[112:115]\n"
    "s_add_u32 s44, s44, s68\n"
    "v_exp_f32 v227, v175\n"
    "v_mfma_f32_16x16x32_bf16 v[112:115], a[212:215], v[188:191], v[112:115]\n"
    "s_add_u32 s49, s49, s67\n"
    "v_cvt_pk_bf16_f32 v199, v226, v227\n"
    "v_mfma_f32_16x16x32_bf16 v[220:223], a[240:243], v[192:195], 0\n"
    "s_add_u32 s55, s55, s66\n"
    "v_mfma_f32_16x16x32_bf16 v[220:223], a[240:243], v[196:199], v[220:223]\n"
    "s_xor_b32 s47, s47, 34816\n"
    "v_mfma_f32_16x16x32_bf16 v[116:119], a[216:219], v[184:187], v[116:119]\n"
    "s_xor_b32 s48, s48, 34816\n"
    "v_exp_f32 v224, v128\n"
    "v_mfma_f32_16x16x32_bf16 v[116:119], a[220:223], v[188:191], v[116:119]\n"
    "s_add_u32 s51, s51, 1\n"
    "v_mfma_f32_16x16x32_bf16 v[120:123], a[224:227], v[184:187], v[120:123]\n"
    "s_cmp_lt_u32 s51, 16\n"
    "v_exp_f32 v225, v129\n"
    "v_mfma_f32_16x16x32_bf16 v[120:123], a[228:231], v[188:191], v[120:123]\n"
    "v_cmp_nge_f32 vcc, s53, v220\n"
    "v_mfma_f32_16x16x32_bf16 v[124:127], a[232:235], v[184:187], v[124:127]\n"
    "v_exp_f32 v226, v130\n"
    "v_mfma_f32_16x16x32_bf16 v[124:127], a[236:239], v[188:191], v[124:127]\n"
    "s_cbranch_vccnz RARE4_%=\n"
    "RET4_%=:\n"
    "v_cvt_pk_bf16_f32 v176, v224, v225\n"
    "s_cbranch_scc1 LOOP_%=\n"
    "POST_%=:\n"
    "s_cmp_eq_u32 s72, 0\n"
    "s_cbranch_scc1 LAST_%=\n"
    "s_mul_i32 s62, s52, 77824\n"
    "s_add_u32 s28, s90, s62\n"
    "s_addc_u32 s29, s91, 0\n"
    "s_waitcnt vmcnt(12)\n"
    "v_mov_b32 v224, 0\n"
    "v_mov_b32 v225, 0\n"
    "v_mov_b32 v226, 0\n"
    "v_mov_b32 v227, 0\n"
    "v_add_f32 v232, v232, v220\n"
    "s_cmp_eq_u32 s40, 3\n"
    "s_cselect_b32 s63, 0x46ea6000, 0\n"
    "s_mov_b32 s38, s57\n"
    "s_add_u32 s67, s54, 16384\n"
    "s_add_u32 s68, s81, s71\n"
    "s_add_u32 m0, s68, 17408\n"
    "s_nop 0\n"
    "buffer_load_dwordx4 v237, s[36:39], s67 offen offset:0 nt lds\n"
    "s_add_u32 m0, s68, 17472\n"
    "s_nop 0\n"
    "buffer_load_dwordx4 v237, s[36:39], s67 offen offset:1024 nt lds\n"
    "s_add_u32 m0, s68, 17536\n"
    "s_nop 0\n"
    "buffer_load_dwordx4 v237, s[36:39], s67 offen offset:2048 nt lds\n"
    "s_add_u32 m0, s68, 17600\n"
    "s_nop 0\n"
    "buffer_load_dwordx4 v237, s[36:39], s67 offen offset:3072 nt lds\n"
    "v_mfma_f32_16x16x32_bf16 a[0:3], a[176:179], v[192:195], a[0:3]\n"
    "v_add_u32 v244, s81, v241\n"
    "v_rcp_f32 v242, v228\n"
    "s_nop 0\n"
    "v_mfma_f32_16x16x32_bf16 a[0:3], a[180:183], v[196:199], a[0:3]\n"
    "v_mov_b32 v243, v242\n"
    "v_pk_mul_f32 v[0:1], v[0:1], v[242:243]\n"
    "v_pk_mul_f32 v[2:3], v[2:3], v[242:243]\n"
    "v_mfma_f32_16x16x32_bf16 a[4:7], a[184:187], v[192:195], a[4:7]\n"
    "v_cvt_pk_bf16_f32 v144, v0, v1\n"
    "v_cvt_pk_bf16_f32 v145, v2, v3\n"
    "v_pk_mul_f32 v[4:5], v[4:5], v[242:243]\n"
    "v_mfma_f32_16x16x32_bf16 a[4:7], a[188:191], v[196:199], a[4:7]\n"
    "v_pk_mul_f32 v[6:7], v[6:7], v[242:243]\n"
    "v_cvt_pk_bf16_f32 v146, v4, v5\n"
    "v_cvt_pk_bf16_f32 v147, v6, v7\n"
    "v_mfma_f32_16x16x32_bf16 a[8:11], a[192:195], v[192:195], a[8:11]\n"
    "v_pk_mul_f32 v[8:9], v[8:9], v[242:243]\n"
    "v_pk_mul_f32 v[10:11], v[10:11], v[242:243]\n"
    "v_cvt_pk_bf16_f32 v148, v8, v9\n"
    "v_mfma_f32_16x16x32_bf16 a[8:11], a[196:199], v[196:199], a[8:11]\n"
    "v_cvt_pk_bf16_f32 v149, v10, v11\n"
    "v_pk_mul_f32 v[12:13], v[12:13], v[242:243]\n"
    "v_pk_mul_f32 v[14:15], v[14:15], v[242:243]\n"
    "v_mfma_f32_16x16x32_bf16 a[12:15], a[200:203], v[192:195], a[12:15]\n"
    "v_cvt_pk_bf16_f32 v150, v12, v13\n"
    "v_cvt_pk_bf16_f32 v151, v14, v15\n"
    "v_pk_mul_f32 v[16:17], v[16:17], v[242:243]\n"
    "v_mfma_f32_16x16x32_bf16 a[12:15], a[204:207], v[196:199], a[12:15]\n"
    "v_pk_mul_f32 v[18:19], v[18:19], v[242:243]\n"
    "v_cvt_pk_bf16_f32 v152, v16, v17\n"
    "v_cvt_pk_bf16_f32 v153, v18, v19\n"
    "v_mfma_f32_16x16x32_bf16 a[16:19], a[208:211], v[192:195], a[16:19]\n"
    "v_pk_mul_f32 v[20:21], v[20:21], v[242:243]\n"
    "v_pk_mul_f32 v[22:23], v[22:23], v[242:243]\n"
    "v_cvt_pk_bf16_f32 v154, v20, v21\n"
    "v_mfma_f32_16x16x32_bf16 a[16:19], a[212:215], v[196:199], a[16:19]\n"
    "v_cvt_pk_bf16_f32 v155, v22, v23\n"
    "v_pk_mul_f32 v[24:25], v[24:25], v[242:243]\n"
    "v_pk_mul_f32 v[26:27], v[26:27], v[242:243]\n"
    "v_mfma_f32_16x16x32_bf16 a[20:23], a[216:219], v[192:195], a[20:23]\n"
    "v_cvt_pk_bf16_f32 v156, v24, v25\n"
    "v_cvt_pk_bf16_f32 v157, v26, v27\n"
    "v_pk_mul_f32 v[28:29], v[28:29], v[242:243]\n"
    "v_mfma_f32_16x16x32_bf16 a[20:23], a[220:223], v[196:199], a[20:23]\n"
    "v_pk_mul_f32 v[30:31], v[30:31], v[242:243]\n"
    "v_cvt_pk_bf16_f32 v158, v28, v29\n"
    "v_cvt_pk_bf16_f32 v159, v30, v31\n"
    "v_mfma_f32_16x16x32_bf16 a[24:27], a[224:227], v[192:195], a[24:27]\n"
    "ds_read_b128 v[0:3], v244 offset:0\n"
    "ds_read_b128 v[4:7], v244 offset:64\n"
    "ds_read_b128 v[8:11], v244 offset:128\n"
    "v_mfma_f32_16x16x32_bf16 a[24:27], a[228:231], v[196:199], a[24:27]\n"
    "ds_read_b128 v[12:15], v244 offset:192\n"
    "ds_read_b128 v[16:19], v244 offset:4352\n"
    "ds_read_b128 v[20:23], v244 offset:4416\n"
    "v_mfma_f32_16x16x32_bf16 a[28:31], a[232:235], v[192:195], a[28:31]\n"
    "ds_read_b128 v[24:27], v244 offset:4480\n"
    "ds_read_b128 v[28:31], v244 offset:4544\n"
    "v_rcp_f32 v242, v229\n"
    "v_mfma_f32_16x16x32_bf16 a[28:31], a[236:239], v[196:199], a[28:31]\n"
    "s_nop 0\n"
    "v_mov_b32 v243, v242\n"
    "v_pk_mul_f32 v[32:33], v[32:33], v[242:243]\n"
    "v_mfma_f32_32x32x16_bf16 a[176:191], v[224:227], v[224:227], 0\n"
    "v_pk_mul_f32 v[34:35], v[34:35], v[242:243]\n"
    "v_cvt_pk_bf16_f32 v160, v32, v33\n"
    "v_cvt_pk_bf16_f32 v161, v34, v35\n"
    "v_mfma_f32_32x32x16_bf16 a[192:207], v[224:227], v[224:227], 0\n"
    "v_pk_mul_f32 v[36:37], v[36:37], v[242:243]\n"
    "v_pk_mul_f32 v[38:39], v[38:39], v[242:243]\n"
    "v_cvt_pk_bf16_f32 v162, v36, v37\n"
    "v_mfma_f32_32x32x16_bf16 a[208:223], v[224:227], v[224:227], 0\n"
    "v_cvt_pk_bf16_f32 v163, v38, v39\n"
    "v_pk_mul_f32 v[40:41], v[40:41], v[242:243]\n"
    "v_pk_mul_f32 v[42:43], v[42:43], v[242:243]\n"
    "v_mfma_f32_32x32x16_bf16 a[224:239], v[224:227], v[224:227], 0\n"
    "v_cvt_pk_bf16_f32 v164, v40, v41\n"
    "v_cvt_pk_bf16_f32 v165, v42, v43\n"
    "v_pk_mul_f32 v[44:45], v[44:45], v[242:243]\n"
    "v_pk_mul_f32 v[46:47], v[46:47], v[242:243]\n"
    "v_cvt_pk_bf16_f32 v166, v44, v45\n"
    "v_cvt_pk_bf16_f32 v167, v46, v47\n"
    "v_pk_mul_f32 v[48:49], v[48:49], v[242:243]\n"
    "v_pk_mul_f32 v[50:51], v[50:51], v[242:243]\n"
    "v_cvt_pk_bf16_f32 v168, v48, v49\n"
    "v_cvt_pk_bf16_f32 v169, v50, v51\n"
    "v_pk_mul_f32 v[52:53], v[52:53], v[242:243]\n"
    "v_pk_mul_f32 v[54:55], v[54:55], v[242:243]\n"
    "v_cvt_pk_bf16_f32 v170, v52, v53\n"
    "v_cvt_pk_bf16_f32 v171, v54, v55\n"
    "v_pk_mul_f32 v[56:57], v[56:57], v[242:243]\n"
    "v_pk_mul_f32 v[58:59], v[58:59], v[242:243]\n"
    "v_cvt_pk_bf16_f32 v172, v56, v57\n"
    "v_cvt_pk_bf16_f32 v173, v58, v59\n"
    "v_pk_mul_f32 v[60:61], v[60:61], v[242:243]\n"
    "v_pk_mul_f32 v[62:63], v[62:63], v[242:243]\n"
    "v_cvt_pk_bf16_f32 v174, v60, v61\n"
    "v_cvt_pk_bf16_f32 v175, v62, v63\n"
    "ds_read_b128 v[32:35], v244 offset:8704\n"
    "ds_read_b128 v[36:39], v244 offset:8768\n"
    "ds_read_b128 v[40:43], v244 offset:8832\n"
    "ds_read_b128 v[44:47], v244 offset:8896\n"
    "ds_read_b128 v[48:51], v244 offset:13056\n"
    "ds_read_b128 v[52:55], v244 offset:13120\n"
    "ds_read_b128 v[56:59], v244 offset:13184\n"
    "ds_read_b128 v[60:63], v244 offset:13248\n"
    "s_waitcnt lgkmcnt(8)\n"
    "ds_write_b64 v235, v[144:145] offset:0\n"
    "ds_write_b64 v235, v[146:147] offset:32\n"
    "ds_write_b64 v235, v[148:149] offset:64\n"
    "ds_write_b64 v235, v[150:151] offset:96\n"
    "ds_write_b64 v235, v[152:153] offset:128\n"
    "ds_write_b64 v235, v[154:155] offset:160\n"
    "ds_write_b64 v235, v[156:157] offset:192\n"
    "ds_write_b64 v235, v[158:159] offset:224\n"
    "ds_write_b64 v235, v[160:161] offset:4352\n"
    "ds_write_b64 v235, v[162:163] offset:4384\n"
    "ds_write_b64 v235, v[164:165] offset:4416\n"
    "ds_write_b64 v235, v[166:167] offset:4448\n"
    "ds_write_b64 v235, v[168:169] offset:4480\n"
    "ds_write_b64 v235, v[170:171] offset:4512\n"
    "ds_write_b64 v235, v[172:173] offset:4544\n"
    "ds_write_b64 v235, v[174:175] offset:4576\n"
    "v_lshlrev_b32 v192, 16, v0\n"
    "v_and_b32 v193, s73, v0\n"
    "v_lshlrev_b32 v194, 16, v1\n"
    "v_and_b32 v195, s73, v1\n"
    "v_lshlrev_b32 v196, 16, v2\n"
    "v_and_b32 v197, s73, v2\n"
    "v_lshlrev_b32 v198, 16, v3\n"
    "v_and_b32 v199, s73, v3\n"
    "v_pk_mul_f32 v[192:193], v[192:193], s[58:59]\n"
    "v_pk_mul_f32 v[194:195], v[194:195], s[58:59]\n"
    "v_pk_mul_f32 v[196:197], v[196:197], s[58:59]\n"
    "v_pk_mul_f32 v[198:199], v[198:199], s[58:59]\n"
    "v_cvt_pk_bf16_f32 v0, v192, v193\n"
    "v_cvt_pk_bf16_f32 v1, v194, v195\n"
    "v_cvt_pk_bf16_f32 v2, v196, v197\n"
    "v_cvt_pk_bf16_f32 v3, v198, v199\n"
    "v_accvgpr_write_b32 a32, v0\n"
    "v_accvgpr_write_b32 a33, v1\n"
    "v_accvgpr_write_b32 a34, v2\n"
    "v_accvgpr_write_b32 a35, v3\n"
    "v_lshlrev_b32 v192, 16, v4\n"
    "v_and_b32 v193, s73, v4\n"
    "v_lshlrev_b32 v194, 16, v5\n"
    "v_and_b32 v195, s73, v5\n"
    "v_lshlrev_b32 v196, 16, v6\n"
    "v_and_b32 v197, s73, v6\n"
    "v_lshlrev_b32 v198, 16, v7\n"
    "v_and_b32 v199, s73, v7\n"
    "v_pk_mul_f32 v[192:193], v[192:193], s[58:59]\n"
    "v_pk_mul_f32 v[194:195], v[194:195], s[58:59]\n"
    "v_pk_mul_f32 v[196:197], v[196:197], s[58:59]\n"
    "v_pk_mul_f32 v[198:199], v[198:199], s[58:59]\n"
    "v_cvt_pk_bf16_f32 v4, v192, v193\n"
    "v_cvt_pk_bf16_f32 v5, v194, v195\n"
    "v_cvt_pk_bf16_f32 v6, v196, v197\n"
    "v_cvt_pk_bf16_f32 v7, v198, v199\n"
    "v_accvgpr_write_b32 a36, v4\n"
    "v_accvgpr_write_b32 a37, v5\n"
    "v_accvgpr_write_b32 a38, v6\n"
    "v_accvgpr_write_b32 a39, v7\n"
    "v_lshlrev_b32 v192, 16, v8\n"
    "v_and_b32 v193, s73, v8\n"
    "v_lshlrev_b32 v194, 16, v9\n"
    "v_and_b32 v195, s73, v9\n"
    "v_lshlrev_b32 v196, 16, v10\n"
    "v_and_b32 v197, s73, v10\n"
    "v_lshlrev_b32 v198, 16, v11\n"
    "v_and_b32 v199, s73, v11\n"
    "v_pk_mul_f32 v[192:193], v[192:193], s[58:59]\n"
    "v_pk_mul_f32 v[194:195], v[194:195], s[58:59]\n"
    "v_pk_mul_f32 v[196:197], v[196:197], s[58:59]\n"
    "v_pk_mul_f32 v[198:199], v[198:199], s[58:59]\n"
    "v_cvt_pk_bf16_f32 v8, v192, v193\n"
    "v_cvt_pk_bf16_f32 v9, v194, v195\n"
    "v_cvt_pk_bf16_f32 v10, v196, v197\n"
    "v_cvt_pk_bf16_f32 v11, v198, v199\n"
    "v_accvgpr_write_b32 a40, v8\n"
    "v_accvgpr_write_b32 a41, v9\n"
    "v_accvgpr_write_b32 a42, v10\n"
    "v_accvgpr_write_b32 a43, v11\n"
    "v_lshlrev_b32 v192, 16, v12\n"
    "v_and_b32 v193, s73, v12\n"
    "v_lshlrev_b32 v194, 16, v13\n"
    "v_and_b32 v195, s73, v13\n"
    "v_lshlrev_b32 v196, 16, v14\n"
    "v_and_b32 v197, s73, v14\n"
    "v_lshlrev_b32 v198, 16, v15\n"
    "v_and_b32 v199, s73, v15\n"
    "v_pk_mul_f32 v[192:193], v[192:193], s[58:59]\n"
    "v_pk_mul_f32 v[194:195], v[194:195], s[58:59]\n"
    "v_pk_mul_f32 v[196:197], v[196:197], s[58:59]\n"
    "v_pk_mul_f32 v[198:199], v[198:199], s[58:59]\n"
    "v_cvt_pk_bf16_f32 v12, v192, v193\n"
    "v_cvt_pk_bf16_f32 v13, v194, v195\n"
    "v_cvt_pk_bf16_f32 v14, v196, v197\n"
    "v_cvt_pk_bf16_f32 v15, v198, v199\n"
    "v_accvgpr_write_b32 a44, v12\n"
    "v_accvgpr_write_b32 a45, v13\n"
    "v_accvgpr_write_b32 a46, v14\n"
    "v_accvgpr_write_b32 a47, v15\n"
    "v_rcp_f32 v242, v230\n"
    "s_nop 0\n"
    "v_mfma_f32_16x16x32_bf16 v[200:203], a[112:115], a[32:35], 0\n"
    "v_mov_b32 v243, v242\n"
    "v_pk_mul_f32 v[64:65], v[64:65], v[242:243]\n"
    "v_mfma_f32_16x16x32_bf16 v[200:203], a[116:119], a[36:39], v[200:203]\n"
    "v_pk_mul_f32 v[66:67], v[66:67], v[242:243]\n"
    "v_cvt_pk_bf16_f32 v144, v64, v65\n"
    "v_mfma_f32_16x16x32_bf16 v[200:203], a[120:123], a[40:43], v[200:203]\n"
    "v_cvt_pk_bf16_f32 v145, v66, v67\n"
    "v_pk_mul_f32 v[68:69], v[68:69], v[242:243]\n"
    "v_mfma_f32_16x16x32_bf16 v[200:203], a[124:127], a[44:47], v[200:203]\n"
    "v_pk_mul_f32 v[70:71], v[70:71], v[242:243]\n"
    "v_cvt_pk_bf16_f32 v146, v68, v69\n"
    "v_cvt_pk_bf16_f32 v147, v70, v71\n"
    "v_pk_mul_f32 v[72:73], v[72:73], v[242:243]\n"
    "v_pk_mul_f32 v[74:75], v[74:75], v[242:243]\n"
    "v_cvt_pk_bf16_f32 v148, v72, v73\n"
    "v_cvt_pk_bf16_f32 v149, v74, v75\n"
    "v_pk_mul_f32 v[76:77], v[76:77], v[242:243]\n"
    "v_pk_mul_f32 v[78:79], v[78:79], v[242:243]\n"
    "v_cvt_pk_bf16_f32 v150, v76, v77\n"
    "v_cvt_pk_bf16_f32 v151, v78, v79\n"
    "v_pk_mul_f32 v[80:81], v[80:81], v[242:243]\n"
    "v_pk_mul_f32 v[82:83], v[82:83], v[242:243]\n"
    "v_cvt_pk_bf16_f32 v152, v80, v81\n"
    "v_cvt_pk_bf16_f32 v153, v82, v83\n"
    "v_pk_mul_f32 v[84:85], v[84:85], v[242:243]\n"
    "v_max3_f32 v245, v200, v201, v202\n"
    "v_pk_mul_f32 v[86:87], v[86:87], v[242:243]\n"
    "v_cvt_pk_bf16_f32 v154, v84, v85\n"
    "v_cvt_pk_bf16_f32 v155, v86, v87\n"
    "v_max_f32 v245, v245, v203\n"
    "v_pk_mul_f32 v[88:89], v[88:89], v[242:243]\n"
    "v_pk_mul_f32 v[90:91], v[90:91], v[242:243]\n"
    "v_cvt_pk_bf16_f32 v156, v88, v89\n"
    "v_mov_b32 v246, v245\n"
    "v_cvt_pk_bf16_f32 v157, v90, v91\n"
    "v_pk_mul_f32 v[92:93], v[92:93], v[242:243]\n"
    "v_pk_mul_f32 v[94:95], v[94:95], v[242:243]\n"
    "v_permlane16_swap_b32 v245, v246\n"
    "v_cvt_pk_bf16_f32 v158, v92, v93\n"
    "v_cvt_pk_bf16_f32 v159, v94, v95\n"
    "v_lshlrev_b32 v192, 16, v16\n"
    "v_max_f32 v245, v245, v246\n"
    "v_and_b32 v193, s73, v16\n"
    "v_lshlrev_b32 v194, 16, v17\n"
    "v_and_b32 v195, s73, v17\n"
    "v_mov_b32 v246, v245\n"
    "v_lshlrev_b32 v196, 16, v18\n"
    "v_and_b32 v197, s73, v18\n"
    "v_lshlrev_b32 v198, 16, v19\n"
    "v_permlane32_swap_b32 v245, v246\n"
    "v_and_b32 v199, s73, v19\n"
    "v_pk_mul_f32 v[192:193], v[192:193], s[58:59]\n"
    "v_pk_mul_f32 v[194:195], v[194:195], s[58:59]\n"
    "v_max_f32 v245, v245, v246\n"
    "v_pk_mul_f32 v[196:197], v[196:197], s[58:59]\n"
    "v_pk_mul_f32 v[198:199], v[198:199], s[58:59]\n"
    "v_cvt_pk_bf16_f32 v16, v192, v193\n"
    "v_sub_f32 v200, 0, v245\n"
    "v_cvt_pk_bf16_f32 v17, v194, v195\n"
    "v_cvt_pk_bf16_f32 v18, v196, v197\n"
    "v_cvt_pk_bf16_f32 v19, v198, v199\n"
    "v_sub_f32 v201, 0, v245\n"
    "v_accvgpr_write_b32 a48, v16\n"
    "v_accvgpr_write_b32 a49, v17\n"
    "v_accvgpr_write_b32 a50, v18\n"
    "v_sub_f32 v202, 0, v245\n"
    "v_accvgpr_write_b32 a51, v19\n"
    "v_lshlrev_b32 v192, 16, v20\n"
    "v_and_b32 v193, s73, v20\n"
    "v_sub_f32 v203, 0, v245\n"
    "v_lshlrev_b32 v194, 16, v21\n"
    "v_and_b32 v195, s73, v21\n"
    "v_lshlrev_b32 v196, 16, v22\n"
    "v_and_b32 v197, s73, v22\n"
    "v_lshlrev_b32 v198, 16, v23\n"
    "v_and_b32 v199, s73, v23\n"
    "v_pk_mul_f32 v[192:193], v[192:193], s[58:59]\n"
    "v_pk_mul_f32 v[194:195], v[194:195], s[58:59]\n"
    "v_pk_mul_f32 v[196:197], v[196:197], s[58:59]\n"
    "v_pk_mul_f32 v[198:199], v[198:199], s[58:59]\n"
    "v_cvt_pk_bf16_f32 v20, v192, v193\n"
    "v_cvt_pk_bf16_f32 v21, v194, v195\n"
    "v_cvt_pk_bf16_f32 v22, v196, v197\n"
    "v_mfma_f32_16x16x32_bf16 v[128:131], a[112:115], a[32:35], v[200:203]\n"
    "v_cvt_pk_bf16_f32 v23, v198, v199\n"
    "v_accvgpr_write_b32 a52, v20\n"
    "v_mfma_f32_16x16x32_bf16 v[128:131], a[116:119], a[36:39], v[128:131]\n"
    "v_accvgpr_write_b32 a53, v21\n"
    "v_accvgpr_write_b32 a54, v22\n"
    "v_mfma_f32_16x16x32_bf16 v[128:131], a[120:123], a[40:43], v[128:131]\n"
    "v_accvgpr_write_b32 a55, v23\n"
    "v_lshlrev_b32 v192, 16, v24\n"
    "v_mfma_f32_16x16x32_bf16 v[128:131], a[124:127], a[44:47], v[128:131]\n"
    "v_and_b32 v193, s73, v24\n"
    "v_lshlrev_b32 v194, 16, v25\n"
    "v_mfma_f32_16x16x32_bf16 v[132:135], a[128:131], a[32:35], v[200:203]\n"
    "v_and_b32 v195, s73, v25\n"
    "v_lshlrev_b32 v196, 16, v26\n"
    "v_mfma_f32_16x16x32_bf16 v[132:135], a[132:135], a[36:39], v[132:135]\n"
    "v_and_b32 v197, s73, v26\n"
    "v_lshlrev_b32 v198, 16, v27\n"
    "v_mfma_f32_16x16x32_bf16 v[132:135], a[136:139], a[40:43], v[132:135]\n"
    "v_and_b32 v199, s73, v27\n"
    "v_pk_mul_f32 v[192:193], v[192:193], s[58:59]\n"
    "v_mfma_f32_16x16x32_bf16 v[132:135], a[140:143], a[44:47], v[132:135]\n"
    "v_pk_mul_f32 v[194:195], v[194:195], s[58:59]\n"
    "v_pk_mul_f32 v[196:197], v[196:197], s[58:59]\n"
    "v_mfma_f32_16x16x32_bf16 v[136:139], a[144:147], a[32:35], v[200:203]\n"
    "v_pk_mul_f32 v[198:199], v[198:199], s[58:59]\n"
    "v_cvt_pk_bf16_f32 v24, v192, v193\n"
    "v_mfma_f32_16x16x32_bf16 v[136:139], a[148:151], a[36:39], v[136:139]\n"
    "v_cvt_pk_bf16_f32 v25, v194, v195\n"
    "v_cvt_pk_bf16_f32 v26, v196, v197\n"
    "v_mfma_f32_16x16x32_bf16 v[136:139], a[152:155], a[40:43], v[136:139]\n"
    "v_cvt_pk_bf16_f32 v27, v198, v199\n"
    "v_accvgpr_write_b32 a56, v24\n"
    "v_mfma_f32_16x16x32_bf16 v[136:139], a[156:159], a[44:47], v[136:139]\n"
    "v_accvgpr_write_b32 a57, v25\n"
    "v_accvgpr_write_b32 a58, v26\n"
    "v_mfma_f32_16x16x32_bf16 v[140:143], a[160:163], a[32:35], v[200:203]\n"
    "v_accvgpr_write_b32 a59, v27\n"
    "v_lshlrev_b32 v192, 16, v28\n"
    "v_mfma_f32_16x16x32_bf16 v[140:143], a[164:167], a[36:39], v[140:143]\n"
    "v_and_b32 v193, s73, v28\n"
    "v_lshlrev_b32 v194, 16, v29\n"
    "v_mfma_f32_16x16x32_bf16 v[140:143], a[168:171], a[40:43], v[140:143]\n"
    "v_and_b32 v195, s73, v29\n"
    "v_lshlrev_b32 v196, 16, v30\n"
    "v_mfma_f32_16x16x32_bf16 v[140:143], a[172:175], a[44:47], v[140:143]\n"
    "v_and_b32 v197, s73, v30\n"
    "v_lshlrev_b32 v198, 16, v31\n"
    "v_and_b32 v199, s73, v31\n"
    "v_pk_mul_f32 v[192:193], v[192:193], s[58:59]\n"
    "v_pk_mul_f32 v[194:195], v[194:195], s[58:59]\n"
    "v_pk_mul_f32 v[196:197], v[196:197], s[58:59]\n"
    "v_pk_mul_f32 v[198:199], v[198:199], s[58:59]\n"
    "v_cvt_pk_bf16_f32 v28, v192, v193\n"
    "v_cvt_pk_bf16_f32 v29, v194, v195\n"
    "v_cvt_pk_bf16_f32 v30, v196, v197\n"
    "v_cvt_pk_bf16_f32 v31, v198, v199\n"
    "v_accvgpr_write_b32 a60, v28\n"
    "v_accvgpr_write_b32 a61, v29\n"
    "v_accvgpr_write_b32 a62, v30\n"
    "v_accvgpr_write_b32 a63, v31\n"
    "s_waitcnt lgkmcnt(0)\n"
    "ds_write_b64 v235, v[144:145] offset:8704\n"
    "v_mfma_f32_16x16x32_bf16 v[204:207], a[112:115], a[48:51], 0\n"
    "ds_write_b64 v235, v[146:147] offset:8736\n"
    "ds_write_b64 v235, v[148:149] offset:8768\n"
    "v_mfma_f32_16x16x32_bf16 v[204:207], a[116:119], a[52:55], v[204:207]\n"
    "ds_write_b64 v235, v[150:151] offset:8800\n"
    "ds_write_b64 v235, v[152:153] offset:8832\n"
    "v_mfma_f32_16x16x32_bf16 v[204:207], a[120:123], a[56:59], v[204:207]\n"
    "ds_write_b64 v235, v[154:155] offset:8864\n"
    "ds_write_b64 v235, v[156:157] offset:8896\n"
    "v_mfma_f32_16x16x32_bf16 v[204:207], a[124:127], a[60:63], v[204:207]\n"
    "ds_write_b64 v235, v[158:159] offset:8928\n"
    "v_rcp_f32 v242, v231\n"
    "s_nop 0\n"
    "v_mov_b32 v243, v242\n"
    "v_mfma_f32_32x32x16_bf16 v[0:15], v[224:227], v[224:227], 0\n"
    "v_pk_mul_f32 v[96:97], v[96:97], v[242:243]\n"
    "v_pk_mul_f32 v[98:99], v[98:99], v[242:243]\n"
    "v_cvt_pk_bf16_f32 v160, v96, v97\n"
    "v_cvt_pk_bf16_f32 v161, v98, v99\n"
    "v_pk_mul_f32 v[100:101], v[100:101], v[242:243]\n"
    "v_pk_mul_f32 v[102:103], v[102:103], v[242:243]\n"
    "v_mfma_f32_32x32x16_bf16 v[16:31], v[224:227], v[224:227], 0\n"
    "v_cvt_pk_bf16_f32 v162, v100, v101\n"
    "v_cvt_pk_bf16_f32 v163, v102, v103\n"
    "v_pk_mul_f32 v[104:105], v[104:105], v[242:243]\n"
    "v_pk_mul_f32 v[106:107], v[106:107], v[242:243]\n"
    "v_cvt_pk_bf16_f32 v164, v104, v105\n"
    "v_cvt_pk_bf16_f32 v165, v106, v107\n"
    "v_max3_f32 v247, v204, v205, v206\n"
    "v_pk_mul_f32 v[108:109], v[108:109], v[242:243]\n"
    "v_pk_mul_f32 v[110:111], v[110:111], v[242:243]\n"
    "v_cvt_pk_bf16_f32 v166, v108, v109\n"
    "v_max_f32 v247, v247, v207\n"
    "v_cvt_pk_bf16_f32 v167, v110, v111\n"
    "v_pk_mul_f32 v[112:113], v[112:113], v[242:243]\n"
    "v_pk_mul_f32 v[114:115], v[114:115], v[242:243]\n"
    "v_mov_b32 v248, v247\n"
    "v_cvt_pk_bf16_f32 v168, v112, v113\n"
    "v_cvt_pk_bf16_f32 v169, v114, v115\n"
    "v_pk_mul_f32 v[116:117], v[116:117], v[242:243]\n"
    "v_permlane16_swap_b32 v247, v248\n"
    "v_pk_mul_f32 v[118:119], v[118:119], v[242:243]\n"
    "v_cvt_pk_bf16_f32 v170, v116, v117\n"
    "v_cvt_pk_bf16_f32 v171, v118, v119\n"
    "v_max_f32 v247, v247, v248\n"
    "v_pk_mul_f32 v[120:121], v[120:121], v[242:243]\n"
    "v_pk_mul_f32 v[122:123], v[122:123], v[242:243]\n"
    "v_cvt_pk_bf16_f32 v172, v120, v121\n"
    "v_mov_b32 v248, v247\n"
    "v_cvt_pk_bf16_f32 v173, v122, v123\n"
    "v_pk_mul_f32 v[124:125], v[124:125], v[242:243]\n"
    "v_pk_mul_f32 v[126:127], v[126:127], v[242:243]\n"
    "v_permlane32_swap_b32 v247, v248\n"
    "v_cvt_pk_bf16_f32 v174, v124, v125\n"
    "v_cvt_pk_bf16_f32 v175, v126, v127\n"
    "ds_write_b64 v235, v[160:161] offset:13056\n"
    "v_max_f32 v247, v247, v248\n"
    "ds_write_b64 v235, v[162:163] offset:13088\n"
    "ds_write_b64 v235, v[164:165] offset:13120\n"
    "ds_write_b64 v235, v[166:167] offset:13152\n"
    "v_sub_f32 v204, 0, v247\n"
    "ds_write_b64 v235, v[168:169] offset:13184\n"
    "ds_write_b64 v235, v[170:171] offset:13216\n"
    "ds_write_b64 v235, v[172:173] offset:13248\n"
    "v_sub_f32 v205, 0, v247\n"
    "ds_write_b64 v235, v[174:175] offset:13280\n"
    "v_lshlrev_b32 v192, 16, v32\n"
    "v_and_b32 v193, s73, v32\n"
    "v_sub_f32 v206, 0, v247\n"
    "v_lshlrev_b32 v194, 16, v33\n"
    "v_and_b32 v195, s73, v33\n"
    "v_lshlrev_b32 v196, 16, v34\n"
    "v_sub_f32 v207, 0, v247\n"
    "v_and_b32 v197, s73, v34\n"
    "v_lshlrev_b32 v198, 16, v35\n"
    "v_and_b32 v199, s73, v35\n"
    "v_pk_mul_f32 v[192:193], v[192:193], s[58:59]\n"
    "v_pk_mul_f32 v[194:195], v[194:195], s[58:59]\n"
    "v_pk_mul_f32 v[196:197], v[196:197], s[58:59]\n"
    "v_pk_mul_f32 v[198:199], v[198:199], s[58:59]\n"
    "v_cvt_pk_bf16_f32 v32, v192, v193\n"
    "v_cvt_pk_bf16_f32 v33, v194, v195\n"
    "v_cvt_pk_bf16_f32 v34, v196, v197\n"
    "v_cvt_pk_bf16_f32 v35, v198, v199\n"
    "v_accvgpr_write_b32 a64, v32\n"
    "v_accvgpr_write_b32 a65, v33\n"
    "v_accvgpr_write_b32 a66, v34\n"
    "v_accvgpr_write_b32 a67, v35\n"
    "v_lshlrev_b32 v192, 16, v36\n"
    "v_and_b32 v193, s73, v36\n"
    "v_lshlrev_b32 v194, 16, v37\n"
    "v_and_b32 v195, s73, v37\n"
    "v_lshlrev_b32 v196, 16, v38\n"
    "v_and_b32 v197, s73, v38\n"
    "v_lshlrev_b32 v198, 16, v39\n"
    "v_and_b32 v199, s73, v39\n"
    "v_pk_mul_f32 v[192:193], v[192:193], s[58:59]\n"
    "v_pk_mul_f32 v[194:195], v[194:195], s[58:59]\n"
    "s_waitcnt lgkmcnt(0)\n"
    "s_add_u32 s62, s81, 13056\n"
    "s_add_u32 s62, s62, s71\n"
    "v_add_u32 v245, s62, v239\n"
    "ds_read_b128 v[96:99], v245 offset:0\n"
    "ds_read_b128 v[100:103], v245 offset:1088\n"
    "ds_read_b128 v[104:107], v245 offset:2176\n"
    "ds_read_b128 v[108:111], v245 offset:3264\n"
    "v_pk_mul_f32 v[196:197], v[196:197], s[58:59]\n"
    "v_pk_mul_f32 v[198:199], v[198:199], s[58:59]\n"
    "v_cvt_pk_bf16_f32 v36, v192, v193\n"
    "v_cvt_pk_bf16_f32 v37, v194, v195\n"
    "v_cvt_pk_bf16_f32 v38, v196, v197\n"
    "v_cvt_pk_bf16_f32 v39, v198, v199\n"
    "v_accvgpr_write_b32 a68, v36\n"
    "v_accvgpr_write_b32 a69, v37\n"
    "v_accvgpr_write_b32 a70, v38\n"
    "v_accvgpr_write_b32 a71, v39\n"
    "v_lshlrev_b32 v192, 16, v40\n"
    "v_and_b32 v193, s73, v40\n"
    "v_lshlrev_b32 v194, 16, v41\n"
    "v_and_b32 v195, s73, v41\n"
    "v_lshlrev_b32 v196, 16, v42\n"
    "v_and_b32 v197, s73, v42\n"
    "v_lshlrev_b32 v198, 16, v43\n"
    "v_and_b32 v199, s73, v43\n"
    "v_pk_mul_f32 v[192:193], v[192:193], s[58:59]\n"
    "v_pk_mul_f32 v[194:195], v[194:195], s[58:59]\n"
    "v_pk_mul_f32 v[196:197], v[196:197], s[58:59]\n"
    "v_pk_mul_f32 v[198:199], v[198:199], s[58:59]\n"
    "v_cvt_pk_bf16_f32 v40, v192, v193\n"
    "v_cvt_pk_bf16_f32 v41, v194, v195\n"
    "v_cvt_pk_bf16_f32 v42, v196, v197\n"
    "v_cvt_pk_bf16_f32 v43, v198, v199\n"
    "v_accvgpr_write_b32 a72, v40\n"
    "v_accvgpr_write_b32 a73, v41\n"
    "v_accvgpr_write_b32 a74, v42\n"
    "v_accvgpr_write_b32 a75, v43\n"
    "v_lshlrev_b32 v192, 16, v44\n"
    "v_and_b32 v193, s73, v44\n"
    "v_lshlrev_b32 v194, 16, v45\n"
    "v_and_b32 v195, s73, v45\n"
    "v_lshlrev_b32 v196, 16, v46\n"
    "v_and_b32 v197, s73, v46\n"
    "v_lshlrev_b32 v198, 16, v47\n"
    "v_and_b32 v199, s73, v47\n"
    "v_pk_mul_f32 v[192:193], v[192:193], s[58:59]\n"
    "v_pk_mul_f32 v[194:195], v[194:195], s[58:59]\n"
    "v_pk_mul_f32 v[196:197], v[196:197], s[58:59]\n"
    "v_pk_mul_f32 v[198:199], v[198:199], s[58:59]\n"
    "v_cvt_pk_bf16_f32 v44, v192, v193\n"
    "v_cvt_pk_bf16_f32 v45, v194, v195\n"
    "v_cvt_pk_bf16_f32 v46, v196, v197\n"
    "v_cvt_pk_bf16_f32 v47, v198, v199\n"
    "v_accvgpr_write_b32 a76, v44\n"
    "v_accvgpr_write_b32 a77, v45\n"
    "v_accvgpr_write_b32 a78, v46\n"
    "v_accvgpr_write_b32 a79, v47\n"
    "s_waitcnt lgkmcnt(0)\n"
    "s_mov_b32 s30, 77824\n"
    "s_add_u32 s67, s54, 12288\n"
    "buffer_store_dwordx4 v[96:99], v240, s[28:31], s67 offen offset:0 nt\n"
    "buffer_store_dwordx4 v[100:103], v240, s[28:31], s67 offen offset:1024 nt\n"
    "buffer_store_dwordx4 v[104:107], v240, s[28:31], s67 offen offset:2048 nt\n"
    "buffer_store_dwordx4 v[108:111], v240, s[28:31], s67 offen offset:3072 nt\n"
    "s_waitcnt vmcnt(4)\n"
    "ds_read_b128 v[64:67], v244 offset:17408\n"
    "v_mfma_f32_16x16x32_bf16 v[208:211], a[112:115], a[64:67], 0\n"
    "ds_read_b128 v[68:71], v244 offset:17472\n"
    "ds_read_b128 v[72:75], v244 offset:17536\n"
    "v_mfma_f32_16x16x32_bf16 v[208:211], a[116:119], a[68:71], v[208:211]\n"
    "ds_read_b128 v[76:79], v244 offset:17600\n"
    "v_rcp_f32 v242, v232\n"
    "v_mfma_f32_16x16x32_bf16 v[208:211], a[120:123], a[72:75], v[208:211]\n"
    "s_nop 0\n"
    "v_mov_b32 v243, v242\n"
    "v_mfma_f32_16x16x32_bf16 v[208:211], a[124:127], a[76:79], v[208:211]\n"
    "v_accvgpr_read_b32 v176, a0\n"
    "v_accvgpr_read_b32 v177, a1\n"
    "v_accvgpr_read_b32 v178, a2\n"
    "v_accvgpr_read_b32 v179, a3\n"
    "v_pk_mul_f32 v[176:177], v[176:177], v[242:243]\n"
    "v_pk_mul_f32 v[178:179], v[178:179], v[242:243]\n"
    "v_cvt_pk_bf16_f32 v144, v176, v177\n"
    "v_cvt_pk_bf16_f32 v145, v178, v179\n"
    "v_accvgpr_read_b32 v184, a4\n"
    "v_accvgpr_read_b32 v185, a5\n"
    "v_accvgpr_read_b32 v186, a6\n"
    "v_accvgpr_read_b32 v187, a7\n"
    "v_pk_mul_f32 v[184:185], v[184:185], v[242:243]\n"
    "v_pk_mul_f32 v[186:187], v[186:187], v[242:243]\n"
    "v_cvt_pk_bf16_f32 v146, v184, v185\n"
    "v_cvt_pk_bf16_f32 v147, v186, v187\n"
    "v_max3_f32 v249, v208, v209, v210\n"
    "v_accvgpr_read_b32 v176, a8\n"
    "v_accvgpr_read_b32 v177, a9\n"
    "v_accvgpr_read_b32 v178, a10\n"
    "v_max_f32 v249, v249, v211\n"
    "v_accvgpr_read_b32 v179, a11\n"
    "v_pk_mul_f32 v[176:177], v[176:177], v[242:243]\n"
    "v_pk_mul_f32 v[178:179], v[178:179], v[242:243]\n"
    "v_mov_b32 v250, v249\n"
    "v_cvt_pk_bf16_f32 v148, v176, v177\n"
    "v_cvt_pk_bf16_f32 v149, v178, v179\n"
    "v_accvgpr_read_b32 v184, a12\n"
    "v_permlane16_swap_b32 v249, v250\n"
    "v_accvgpr_read_b32 v185, a13\n"
    "v_accvgpr_read_b32 v186, a14\n"
    "v_accvgpr_read_b32 v187, a15\n"
    "v_max_f32 v249, v249, v250\n"
    "v_pk_mul_f32 v[184:185], v[184:185], v[242:243]\n"
    "v_pk_mul_f32 v[186:187], v[186:187], v[242:243]\n"
    "v_cvt_pk_bf16_f32 v150, v184, v185\n"
    "v_mov_b32 v250, v249\n"
    "v_cvt_pk_bf16_f32 v151, v186, v187\n"
    "v_accvgpr_read_b32 v176, a16\n"
    "v_accvgpr_read_b32 v177, a17\n"
    "v_permlane32_swap_b32 v249, v250\n"
    "v_accvgpr_read_b32 v178, a18\n"
    "v_accvgpr_read_b32 v179, a19\n"
    "v_pk_mul_f32 v[176:177], v[176:177], v[242:243]\n"
    "v_max_f32 v249, v249, v250\n"
    "v_pk_mul_f32 v[178:179], v[178:179], v[242:243]\n"
    "v_cvt_pk_bf16_f32 v152, v176, v177\n"
    "v_cvt_pk_bf16_f32 v153, v178, v179\n"
    "v_sub_f32 v208, 0, v249\n"
    "v_accvgpr_read_b32 v184, a20\n"
    "v_accvgpr_read_b32 v185, a21\n"
    "v_accvgpr_read_b32 v186, a22\n"
    "v_sub_f32 v209, 0, v249\n"
    "v_accvgpr_read_b32 v187, a23\n"
    "v_pk_mul_f32 v[184:185], v[184:185], v[242:243]\n"
    "v_pk_mul_f32 v[186:187], v[186:187], v[242:243]\n"
    "v_sub_f32 v210, 0, v249\n"
    "v_cvt_pk_bf16_f32 v154, v184, v185\n"
    "v_cvt_pk_bf16_f32 v155, v186, v187\n"
    "v_accvgpr_read_b32 v176, a24\n"
    "v_sub_f32 v211, 0, v249\n"
    "v_accvgpr_read_b32 v177, a25\n"
    "v_accvgpr_read_b32 v178, a26\n"
    "v_accvgpr_read_b32 v179, a27\n"
    "v_pk_mul_f32 v[176:177], v[176:177], v[242:243]\n"
    "v_pk_mul_f32 v[178:179], v[178:179], v[242:243]\n"
    "v_cvt_pk_bf16_f32 v156, v176, v177\n"
    "v_cvt_pk_bf16_f32 v157, v178, v179\n"
    "v_accvgpr_read_b32 v184, a28\n"
    "v_accvgpr_read_b32 v185, a29\n"
    "v_accvgpr_read_b32 v186, a30\n"
    "v_accvgpr_read_b32 v187, a31\n"
    "v_pk_mul_f32 v[184:185], v[184:185], v[242:243]\n"
    "v_pk_mul_f32 v[186:187], v[186:187], v[242:243]\n"
    "v_cvt_pk_bf16_f32 v158, v184, v185\n"
    "v_cvt_pk_bf16_f32 v159, v186, v187\n"
    "ds_write_b64 v235, v[144:145] offset:13056\n"
    "ds_write_b64 v235, v[146:147] offset:13088\n"
    "ds_write_b64 v235, v[148:149] offset:13120\n"
    "ds_write_b64 v235, v[150:151] offset:13152\n"
    "ds_write_b64 v235, v[152:153] offset:13184\n"
    "ds_write_b64 v235, v[154:155] offset:13216\n"
    "ds_write_b64 v235, v[156:157] offset:13248\n"
    "ds_write_b64 v235, v[158:159] offset:13280\n"
    "v_lshlrev_b32 v192, 16, v48\n"
    "v_and_b32 v193, s73, v48\n"
    "v_lshlrev_b32 v194, 16, v49\n"
    "v_and_b32 v195, s73, v49\n"
    "v_mfma_f32_32x32x16_bf16 a[0:15], v[224:227], v[224:227], 0\n"
    "v_lshlrev_b32 v196, 16, v50\n"
    "v_and_b32 v197, s73, v50\n"
    "v_lshlrev_b32 v198, 16, v51\n"
    "v_and_b32 v199, s73, v51\n"
    "v_pk_mul_f32 v[192:193], v[192:193], s[58:59]\n"
    "v_pk_mul_f32 v[194:195], v[194:195], s[58:59]\n"
    "v_mfma_f32_32x32x16_bf16 a[16:31], v[224:227], v[224:227], 0\n"
    "v_pk_mul_f32 v[196:197], v[196:197], s[58:59]\n"
    "v_pk_mul_f32 v[198:199], v[198:199], s[58:59]\n"
    "v_cvt_pk_bf16_f32 v48, v192, v193\n"
    "v_cvt_pk_bf16_f32 v49, v194, v195\n"
    "v_cvt_pk_bf16_f32 v50, v196, v197\n"
    "v_cvt_pk_bf16_f32 v51, v198, v199\n"
    "v_accvgpr_write_b32 a80, v48\n"
    "v_accvgpr_write_b32 a81, v49\n"
    "v_accvgpr_write_b32 a82, v50\n"
    "v_accvgpr_write_b32 a83, v51\n"
    "v_lshlrev_b32 v192, 16, v52\n"
    "v_and_b32 v193, s73, v52\n"
    "v_lshlrev_b32 v194, 16, v53\n"
    "v_and_b32 v195, s73, v53\n"
    "v_lshlrev_b32 v196, 16, v54\n"
    "v_and_b32 v197, s73, v54\n"
    "v_lshlrev_b32 v198, 16, v55\n"
    "v_and_b32 v199, s73, v55\n"
    "v_pk_mul_f32 v[192:193], v[192:193], s[58:59]\n"
    "v_pk_mul_f32 v[194:195], v[194:195], s[58:59]\n"
    "s_waitcnt lgkmcnt(0)\n"
    "s_add_u32 s62, s81, 13056\n"
    "s_add_u32 s62, s62, s71\n"
    "v_add_u32 v245, s62, v239\n"
    "ds_read_b128 v[112:115], v245 offset:0\n"
    "ds_read_b128 v[116:119], v245 offset:1088\n"
    "ds_read_b128 v[120:123], v245 offset:2176\n"
    "ds_read_b128 v[124:127], v245 offset:3264\n"
    "v_pk_mul_f32 v[196:197], v[196:197], s[58:59]\n"
    "v_pk_mul_f32 v[198:199], v[198:199], s[58:59]\n"
    "v_cvt_pk_bf16_f32 v52, v192, v193\n"
    "v_cvt_pk_bf16_f32 v53, v194, v195\n"
    "v_cvt_pk_bf16_f32 v54, v196, v197\n"
    "v_cvt_pk_bf16_f32 v55, v198, v199\n"
    "v_accvgpr_write_b32 a84, v52\n"
    "v_accvgpr_write_b32 a85, v53\n"
    "v_accvgpr_write_b32 a86, v54\n"
    "v_accvgpr_write_b32 a87, v55\n"
    "v_lshlrev_b32 v192, 16, v56\n"
    "v_and_b32 v193, s73, v56\n"
    "v_lshlrev_b32 v194, 16, v57\n"
    "v_and_b32 v195, s73, v57\n"
    "v_lshlrev_b32 v196, 16, v58\n"
    "v_and_b32 v197, s73, v58\n"
    "v_lshlrev_b32 v198, 16, v59\n"
    "v_and_b32 v199, s73, v59\n"
    "v_pk_mul_f32 v[192:193], v[192:193], s[58:59]\n"
    "v_pk_mul_f32 v[194:195], v[194:195], s[58:59]\n"
    "v_pk_mul_f32 v[196:197], v[196:197], s[58:59]\n"
    "v_pk_mul_f32 v[198:199], v[198:199], s[58:59]\n"
    "v_cvt_pk_bf16_f32 v56, v192, v193\n"
    "v_cvt_pk_bf16_f32 v57, v194, v195\n"
    "v_cvt_pk_bf16_f32 v58, v196, v197\n"
    "v_cvt_pk_bf16_f32 v59, v198, v199\n"
    "v_accvgpr_write_b32 a88, v56\n"
    "v_accvgpr_write_b32 a89, v57\n"
    "v_accvgpr_write_b32 a90, v58\n"
    "v_accvgpr_write_b32 a91, v59\n"
    "v_lshlrev_b32 v192, 16, v60\n"
    "v_and_b32 v193, s73, v60\n"
    "v_lshlrev_b32 v194, 16, v61\n"
    "v_and_b32 v195, s73, v61\n"
    "v_lshlrev_b32 v196, 16, v62\n"
    "v_and_b32 v197, s73, v62\n"
    "v_lshlrev_b32 v198, 16, v63\n"
    "v_and_b32 v199, s73, v63\n"
    "v_pk_mul_f32 v[192:193], v[192:193], s[58:59]\n"
    "v_pk_mul_f32 v[194:195], v[194:195], s[58:59]\n"
    "v_pk_mul_f32 v[196:197], v[196:197], s[58:59]\n"
    "v_pk_mul_f32 v[198:199], v[198:199], s[58:59]\n"
    "v_cvt_pk_bf16_f32 v60, v192, v193\n"
    "v_cvt_pk_bf16_f32 v61, v194, v195\n"
    "v_cvt_pk_bf16_f32 v62, v196, v197\n"
    "v_cvt_pk_bf16_f32 v63, v198, v199\n"
    "v_accvgpr_write_b32 a92, v60\n"
    "v_accvgpr_write_b32 a93, v61\n"
    "v_accvgpr_write_b32 a94, v62\n"
    "v_accvgpr_write_b32 a95, v63\n"
    "s_waitcnt lgkmcnt(0)\n"
    "s_mov_b32 s30, 77824\n"
    "s_add_u32 s67, s54, 16384\n"
    "buffer_store_dwordx4 v[112:115], v240, s[28:31], s67 offen offset:0 nt\n"
    "buffer_store_dwordx4 v[116:119], v240, s[28:31], s67 offen offset:1024 nt\n"
    "buffer_store_dwordx4 v[120:123], v240, s[28:31], s67 offen offset:2048 nt\n"
    "buffer_store_dwordx4 v[124:127], v240, s[28:31], s67 offen offset:3072 nt\n"
    "v_lshlrev_b32 v192, 16, v64\n"
    "v_and_b32 v193, s73, v64\n"
    "v_mfma_f32_16x16x32_bf16 v[212:215], a[112:115], a[80:83], 0\n"
    "v_lshlrev_b32 v194, 16, v65\n"
    "v_and_b32 v195, s73, v65\n"
    "v_mfma_f32_16x16x32_bf16 v[212:215], a[116:119], a[84:87], v[212:215]\n"
    "v_lshlrev_b32 v196, 16, v66\n"
    "v_and_b32 v197, s73, v66\n"
    "v_mfma_f32_16x16x32_bf16 v[212:215], a[120:123], a[88:91], v[212:215]\n"
    "v_lshlrev_b32 v198, 16, v67\n"
    "v_and_b32 v199, s73, v67\n"
    "v_mfma_f32_16x16x32_bf16 v[212:215], a[124:127], a[92:95], v[212:215]\n"
    "v_pk_mul_f32 v[192:193], v[192:193], s[58:59]\n"
    "v_pk_mul_f32 v[194:195], v[194:195], s[58:59]\n"
    "v_pk_mul_f32 v[196:197], v[196:197], s[58:59]\n"
    "v_pk_mul_f32 v[198:199], v[198:199], s[58:59]\n"
    "v_mfma_f32_32x32x16_bf16 v[32:47], v[224:227], v[224:227], 0\n"
    "v_cvt_pk_bf16_f32 v64, v192, v193\n"
    "v_cvt_pk_bf16_f32 v65, v194, v195\n"
    "v_cvt_pk_bf16_f32 v66, v196, v197\n"
    "v_cvt_pk_bf16_f32 v67, v198, v199\n"
    "v_accvgpr_write_b32 a96, v64\n"
    "v_accvgpr_write_b32 a97, v65\n"
    "v_mfma_f32_32x32x16_bf16 v[48:63], v[224:227], v[224:227], 0\n"
    "v_accvgpr_write_b32 a98, v66\n"
    "v_accvgpr_write_b32 a99, v67\n"
    "v_lshlrev_b32 v192, 16, v68\n"
    "v_and_b32 v193, s73, v68\n"
    "v_lshlrev_b32 v194, 16, v69\n"
    "v_and_b32 v195, s73, v69\n"
    "v_max3_f32 v251, v212, v213, v214\n"
    "v_lshlrev_b32 v196, 16, v70\n"
    "v_and_b32 v197, s73, v70\n"
    "v_lshlrev_b32 v198, 16, v71\n"
    "v_max_f32 v251, v251, v215\n"
    "v_and_b32 v199, s73, v71\n"
    "v_pk_mul_f32 v[192:193], v[192:193], s[58:59]\n"
    "v_pk_mul_f32 v[194:195], v[194:195], s[58:59]\n"
    "v_mov_b32 v252, v251\n"
    "v_mfma_f32_32x32x16_bf16 v[96:111], v[224:227], v[224:227], 0\n"
    "v_pk_mul_f32 v[196:197], v[196:197], s[58:59]\n"
    "v_pk_mul_f32 v[198:199], v[198:199], s[58:59]\n"
    "v_cvt_pk_bf16_f32 v68, v192, v193\n"
    "v_permlane16_swap_b32 v251, v252\n"
    "v_cvt_pk_bf16_f32 v69, v194, v195\n"
    "v_cvt_pk_bf16_f32 v70, v196, v197\n"
    "v_cvt_pk_bf16_f32 v71, v198, v199\n"
    "v_max_f32 v251, v251, v252\n"
    "v_mfma_f32_32x32x16_bf16 v[112:127], v[224:227], v[224:227], 0\n"
    "v_accvgpr_write_b32 a100, v68\n"
    "v_accvgpr_write_b32 a101, v69\n"
    "v_accvgpr_write_b32 a102, v70\n"
    "v_mov_b32 v252, v251\n"
    "v_accvgpr_write_b32 a103, v71\n"
    "v_lshlrev_b32 v192, 16, v72\n"
    "v_and_b32 v193, s73, v72\n"
    "v_permlane32_swap_b32 v251, v252\n"
    "v_lshlrev_b32 v194, 16, v73\n"
    "v_and_b32 v195, s73, v73\n"
    "v_lshlrev_b32 v196, 16, v74\n"
    "v_max_f32 v251, v251, v252\n"
    "v_and_b32 v197, s73, v74\n"
    "v_lshlrev_b32 v198, 16, v75\n"
    "v_and_b32 v199, s73, v75\n"
    "v_sub_f32 v212, 0, v251\n"
    "v_pk_mul_f32 v[192:193], v[192:193], s[58:59]\n"
    "v_pk_mul_f32 v[194:195], v[194:195], s[58:59]\n"
    "v_pk_mul_f32 v[196:197], v[196:197], s[58:59]\n"
    "v_sub_f32 v213, 0, v251\n"
    "v_pk_mul_f32 v[198:199], v[198:199], s[58:59]\n"
    "v_cvt_pk_bf16_f32 v72, v192, v193\n"
    "v_cvt_pk_bf16_f32 v73, v194, v195\n"
    "v_sub_f32 v214, 0, v251\n"
    "v_cvt_pk_bf16_f32 v74, v196, v197\n"
    "v_cvt_pk_bf16_f32 v75, v198, v199\n"
    "v_accvgpr_write_b32 a104, v72\n"
    "v_sub_f32 v215, 0, v251\n"
    "v_accvgpr_write_b32 a105, v73\n"
    "v_accvgpr_write_b32 a106, v74\n"
    "v_accvgpr_write_b32 a107, v75\n"
    "v_lshlrev_b32 v192, 16, v76\n"
    "v_and_b32 v193, s73, v76\n"
    "v_lshlrev_b32 v194, 16, v77\n"
    "v_and_b32 v195, s73, v77\n"
    "v_lshlrev_b32 v196, 16, v78\n"
    "v_and_b32 v197, s73, v78\n"
    "v_lshlrev_b32 v198, 16, v79\n"
    "v_and_b32 v199, s73, v79\n"
    "v_pk_mul_f32 v[192:193], v[192:193], s[58:59]\n"
    "v_pk_mul_f32 v[194:195], v[194:195], s[58:59]\n"
    "v_pk_mul_f32 v[196:197], v[196:197], s[58:59]\n"
    "v_pk_mul_f32 v[198:199], v[198:199], s[58:59]\n"
    "v_cvt_pk_bf16_f32 v76, v192, v193\n"
    "v_cvt_pk_bf16_f32 v77, v194, v195\n"
    "v_cvt_pk_bf16_f32 v78, v196, v197\n"
    "v_cvt_pk_bf16_f32 v79, v198, v199\n"
    "v_accvgpr_write_b32 a108, v76\n"
    "v_accvgpr_write_b32 a109, v77\n"
    "v_accvgpr_write_b32 a110, v78\n"
    "v_accvgpr_write_b32 a111, v79\n"
    "s_mov_b32 s44, s54\n"
    "s_add_u32 s55, s81, s71\n"
    "s_mov_b32 s57, 0\n"
    "s_mov_b32 s60, 77824\n"
    "s_mov_b32 s49, s56\n"
    "s_mov_b32 s50, 0\n"
    "s_mov_b32 s61, 0x7e00\n"
    "s_add_u32 s52, s52, 1\n"
    "s_mov_b32 s72, 0\n"
    "s_nop 1\n"
    "v_mfma_f32_16x16x32_bf16 v[216:219], a[112:115], a[96:99], 0\n"
    "v_mfma_f32_16x16x32_bf16 v[216:219], a[116:119], a[100:103], v[216:219]\n"
    "v_mfma_f32_16x16x32_bf16 v[216:219], a[120:123], a[104:107], v[216:219]\n"
    "v_mfma_f32_16x16x32_bf16 v[216:219], a[124:127], a[108:111], v[216:219]\n"
    "v_mfma_f32_32x32x16_bf16 v[64:79], v[224:227], v[224:227], 0\n"
    "v_mfma_f32_32x32x16_bf16 v[80:95], v[224:227], v[224:227], 0\n"
    "s_nop 3\n"
    "v_mov_b32 v192, 0\n"
    "v_mov_b32 v193, 0\n"
    "v_max3_f32 v245, v216, v217, v218\n"
    "v_mov_b32 v194, 0\n"
    "v_mov_b32 v195, 0\n"
    "v_max_f32 v245, v245, v219\n"
    "v_mov_b32 v196, 0\n"
    "v_mov_b32 v197, 0\n"
    "v_mov_b32 v246, v245\n"
    "v_mov_b32 v198, 0\n"
    "v_mov_b32 v199, 0\n"
    "v_permlane16_swap_b32 v245, v246\n"
    "v_mov_b32 v220, 0\n"
    "v_mov_b32 v228, 0\n"
    "v_max_f32 v245, v245, v246\n"
    "v_mov_b32 v229, 0\n"
    "v_mov_b32 v230, 0\n"
    "v_mov_b32 v246, v245\n"
    "v_mov_b32 v231, 0\n"
    "v_mov_b32 v232, 0\n"
    "v_permlane32_swap_b32 v245, v246\n"
    "s_nop 0\n"
    "s_nop 0\n"
    "v_max_f32 v245, v245, v246\n"
    "s_nop 0\n"
    "s_nop 0\n"
    "v_add_f32 v245, s63, v245\n"
    "s_nop 0\n"
    "s_nop 0\n"
    "v_sub_f32 v216, 0, v245\n"
    "s_nop 0\n"
    "s_nop 0\n"
    "v_sub_f32 v217, 0, v245\n"
    "s_nop 0\n"
    "s_nop 0\n"
    "v_sub_f32 v218, 0, v245\n"
    "s_nop 0\n"
    "s_nop 0\n"
    "v_sub_f32 v219, 0, v245\n"
    "v_exp_f32 v224, v128\n"
    "v_exp_f32 v225, v129\n"
    "v_exp_f32 v226, v130\n"
    "s_mov_b32 s51, 0\n"
    "v_cvt_pk_bf16_f32 v176, v224, v225\n"
    "s_waitcnt lgkmcnt(0)\n"
    "s_branch HEAD_%=\n"
    "LAST_%=:\n"
    "s_mul_i32 s62, s52, 77824\n"
    "s_add_u32 s28, s90, s62\n"
    "s_addc_u32 s29, s91, 0\n"
    "s_mov_b32 s30, 77824\n"
    "v_add_f32 v232, v232, v220\n"
    "v_mfma_f32_16x16x32_bf16 a[0:3], a[176:179], v[192:195], a[0:3]\n"
    "v_rcp_f32 v242, v228\n"
    "s_nop 0\n"
    "v_mfma_f32_16x16x32_bf16 a[0:3], a[180:183], v[196:199], a[0:3]\n"
    "v_mov_b32 v243, v242\n"
    "v_pk_mul_f32 v[0:1], v[0:1], v[242:243]\n"
    "v_mfma_f32_16x16x32_bf16 a[4:7], a[184:187], v[192:195], a[4:7]\n"
    "v_pk_mul_f32 v[2:3], v[2:3], v[242:243]\n"
    "v_cvt_pk_bf16_f32 v128, v0, v1\n"
    "v_mfma_f32_16x16x32_bf16 a[4:7], a[188:191], v[196:199], a[4:7]\n"
    "v_cvt_pk_bf16_f32 v129, v2, v3\n"
    "v_pk_mul_f32 v[4:5], v[4:5], v[242:243]\n"
    "v_mfma_f32_16x16x32_bf16 a[8:11], a[192:195], v[192:195], a[8:11]\n"
    "v_pk_mul_f32 v[6:7], v[6:7], v[242:243]\n"
    "v_cvt_pk_bf16_f32 v130, v4, v5\n"
    "v_mfma_f32_16x16x32_bf16 a[8:11], a[196:199], v[196:199], a[8:11]\n"
    "v_cvt_pk_bf16_f32 v131, v6, v7\n"
    "v_pk_mul_f32 v[8:9], v[8:9], v[242:243]\n"
    "v_mfma_f32_16x16x32_bf16 a[12:15], a[200:203], v[192:195], a[12:15]\n"
    "v_pk_mul_f32 v[10:11], v[10:11], v[242:243]\n"
    "v_cvt_pk_bf16_f32 v132, v8, v9\n"
    "v_mfma_f32_16x16x32_bf16 a[12:15], a[204:207], v[196:199], a[12:15]\n"
    "v_cvt_pk_bf16_f32 v133, v10, v11\n"
    "v_pk_mul_f32 v[12:13], v[12:13], v[242:243]\n"
    "v_mfma_f32_16x16x32_bf16 a[16:19], a[208:211], v[192:195], a[16:19]\n"
    "v_pk_mul_f32 v[14:15], v[14:15], v[242:243]\n"
    "v_cvt_pk_bf16_f32 v134, v12, v13\n"
    "v_mfma_f32_16x16x32_bf16 a[16:19], a[212:215], v[196:199], a[16:19]\n"
    "v_cvt_pk_bf16_f32 v135, v14, v15\n"
    "v_pk_mul_f32 v[16:17], v[16:17], v[242:243]\n"
    "v_mfma_f32_16x16x32_bf16 a[20:23], a[216:219], v[192:195], a[20:23]\n"
    "v_pk_mul_f32 v[18:19], v[18:19], v[242:243]\n"
    "v_cvt_pk_bf16_f32 v136, v16, v17\n"
    "v_mfma_f32_16x16x32_bf16 a[20:23], a[220:223], v[196:199], a[20:23]\n"
    "v_cvt_pk_bf16_f32 v137, v18, v19\n"
    "v_pk_mul_f32 v[20:21], v[20:21], v[242:243]\n"
    "v_mfma_f32_16x16x32_bf16 a[24:27], a[224:227], v[192:195], a[24:27]\n"
    "v_pk_mul_f32 v[22:23], v[22:23], v[242:243]\n"
    "v_cvt_pk_bf16_f32 v138, v20, v21\n"
    "v_mfma_f32_16x16x32_bf16 a[24:27], a[228:231], v[196:199], a[24:27]\n"
    "v_cvt_pk_bf16_f32 v139, v22, v23\n"
    "v_pk_mul_f32 v[24:25], v[24:25], v[242:243]\n"
    "v_mfma_f32_16x16x32_bf16 a[28:31], a[232:235], v[192:195], a[28:31]\n"
    "v_pk_mul_f32 v[26:27], v[26:27], v[242:243]\n"
    "v_cvt_pk_bf16_f32 v140, v24, v25\n"
    "v_mfma_f32_16x16x32_bf16 a[28:31], a[236:239], v[196:199], a[28:31]\n"
    "v_cvt_pk_bf16_f32 v141, v26, v27\n"
    "v_pk_mul_f32 v[28:29], v[28:29], v[242:243]\n"
    "v_pk_mul_f32 v[30:31], v[30:31], v[242:243]\n"
    "v_cvt_pk_bf16_f32 v142, v28, v29\n"
    "v_cvt_pk_bf16_f32 v143, v30, v31\n"
    "ds_write_b64 v235, v[128:129] offset:0\n"
    "ds_write_b64 v235, v[130:131] offset:32\n"
    "ds_write_b64 v235, v[132:133] offset:64\n"
    "ds_write_b64 v235, v[134:135] offset:96\n"
    "ds_write_b64 v235, v[136:137] offset:128\n"
    "ds_write_b64 v235, v[138:139] offset:160\n"
    "ds_write_b64 v235, v[140:141] offset:192\n"
    "ds_write_b64 v235, v[142:143] offset:224\n"
    "v_rcp_f32 v242, v229\n"
    "s_nop 0\n"
    "v_mov_b32 v243, v242\n"
    "v_pk_mul_f32 v[32:33], v[32:33], v[242:243]\n"
    "v_pk_mul_f32 v[34:35], v[34:35], v[242:243]\n"
    "v_cvt_pk_bf16_f32 v144, v32, v33\n"
    "v_cvt_pk_bf16_f32 v145, v34, v35\n"
    "v_pk_mul_f32 v[36:37], v[36:37], v[242:243]\n"
    "v_pk_mul_f32 v[38:39], v[38:39], v[242:243]\n"
    "v_cvt_pk_bf16_f32 v146, v36, v37\n"
    "v_cvt_pk_bf16_f32 v147, v38, v39\n"
    "s_waitcnt lgkmcnt(0)\n"
    "s_add_u32 s62, s81, 0\n"
    "s_add_u32 s62, s62, s71\n"
    "v_add_u32 v244, s62, v239\n"
    "ds_read_b128 v[0:3], v244 offset:0\n"
    "ds_read_b128 v[4:7], v244 offset:1088\n"
    "ds_read_b128 v[8:11], v244 offset:2176\n"
    "ds_read_b128 v[12:15], v244 offset:3264\n"
    "v_pk_mul_f32 v[40:41], v[40:41], v[242:243]\n"
    "v_pk_mul_f32 v[42:43], v[42:43], v[242:243]\n"
    "v_cvt_pk_bf16_f32 v148, v40, v41\n"
    "v_cvt_pk_bf16_f32 v149, v42, v43\n"
    "v_pk_mul_f32 v[44:45], v[44:45], v[242:243]\n"
    "v_pk_mul_f32 v[46:47], v[46:47], v[242:243]\n"
    "v_cvt_pk_bf16_f32 v150, v44, v45\n"
    "v_cvt_pk_bf16_f32 v151, v46, v47\n"
    "v_pk_mul_f32 v[48:49], v[48:49], v[242:243]\n"
    "v_pk_mul_f32 v[50:51], v[50:51], v[242:243]\n"
    "v_cvt_pk_bf16_f32 v152, v48, v49\n"
    "v_cvt_pk_bf16_f32 v153, v50, v51\n"
    "v_pk_mul_f32 v[52:53], v[52:53], v[242:243]\n"
    "v_pk_mul_f32 v[54:55], v[54:55], v[242:243]\n"
    "v_cvt_pk_bf16_f32 v154, v52, v53\n"
    "v_cvt_pk_bf16_f32 v155, v54, v55\n"
    "v_pk_mul_f32 v[56:57], v[56:57], v[242:243]\n"
    "v_pk_mul_f32 v[58:59], v[58:59], v[242:243]\n"
    "v_cvt_pk_bf16_f32 v156, v56, v57\n"
    "v_cvt_pk_bf16_f32 v157, v58, v59\n"
    "v_pk_mul_f32 v[60:61], v[60:61], v[242:243]\n"
    "v_pk_mul_f32 v[62:63], v[62:63], v[242:243]\n"
    "v_cvt_pk_bf16_f32 v158, v60, v61\n"
    "v_cvt_pk_bf16_f32 v159, v62, v63\n"
    "s_waitcnt lgkmcnt(0)\n"
    "s_add_u32 s67, s54, 0\n"
    "buffer_store_dwordx4 v[0:3], v240, s[28:31], s67 offen offset:0 nt\n"
    "buffer_store_dwordx4 v[4:7], v240, s[28:31], s67 offen offset:1024 nt\n"
    "buffer_store_dwordx4 v[8:11], v240, s[28:31], s67 offen offset:2048 nt\n"
    "buffer_store_dwordx4 v[12:15], v240, s[28:31], s67 offen offset:3072 nt\n"
    "ds_write_b64 v235, v[144:145] offset:4352\n"
    "ds_write_b64 v235, v[146:147] offset:4384\n"
    "ds_write_b64 v235, v[148:149] offset:4416\n"
    "ds_write_b64 v235, v[150:151] offset:4448\n"
    "ds_write_b64 v235, v[152:153] offset:4480\n"
    "ds_write_b64 v235, v[154:155] offset:4512\n"
    "ds_write_b64 v235, v[156:157] offset:4544\n"
    "ds_write_b64 v235, v[158:159] offset:4576\n"
    "v_rcp_f32 v242, v230\n"
    "s_nop 0\n"
    "v_mov_b32 v243, v242\n"
    "v_pk_mul_f32 v[64:65], v[64:65], v[242:243]\n"
    "v_pk_mul_f32 v[66:67], v[66:67], v[242:243]\n"
    "v_cvt_pk_bf16_f32 v160, v64, v65\n"
    "v_cvt_pk_bf16_f32 v161, v66, v67\n"
    "v_pk_mul_f32 v[68:69], v[68:69], v[242:243]\n"
    "v_pk_mul_f32 v[70:71], v[70:71], v[242:243]\n"
    "v_cvt_pk_bf16_f32 v162, v68, v69\n"
    "v_cvt_pk_bf16_f32 v163, v70, v71\n"
    "s_waitcnt lgkmcnt(0)\n"
    "s_add_u32 s62, s81, 4352\n"
    "s_add_u32 s62, s62, s71\n"
    "v_add_u32 v244, s62, v239\n"
    "ds_read_b128 v[16:19], v244 offset:0\n"
    "ds_read_b128 v[20:23], v244 offset:1088\n"
    "ds_read_b128 v[24:27], v244 offset:2176\n"
    "ds_read_b128 v[28:31], v244 offset:3264\n"
    "v_pk_mul_f32 v[72:73], v[72:73], v[242:243]\n"
    "v_pk_mul_f32 v[74:75], v[74:75], v[242:243]\n"
    "v_cvt_pk_bf16_f32 v164, v72, v73\n"
    "v_cvt_pk_bf16_f32 v165, v74, v75\n"
    "v_pk_mul_f32 v[76:77], v[76:77], v[242:243]\n"
    "v_pk_mul_f32 v[78:79], v[78:79], v[242:243]\n"
    "v_cvt_pk_bf16_f32 v166, v76, v77\n"
    "v_cvt_pk_bf16_f32 v167, v78, v79\n"
    "v_pk_mul_f32 v[80:81], v[80:81], v[242:243]\n"
    "v_pk_mul_f32 v[82:83], v[82:83], v[242:243]\n"
    "v_cvt_pk_bf16_f32 v168, v80, v81\n"
    "v_cvt_pk_bf16_f32 v169, v82, v83\n"
    "v_pk_mul_f32 v[84:85], v[84:85], v[242:243]\n"
    "v_pk_mul_f32 v[86:87], v[86:87], v[242:243]\n"
    "v_cvt_pk_bf16_f32 v170, v84, v85\n"
    "v_cvt_pk_bf16_f32 v171, v86, v87\n"
    "v_pk_mul_f32 v[88:89], v[88:89], v[242:243]\n"
    "v_pk_mul_f32 v[90:91], v[90:91], v[242:243]\n"
    "v_cvt_pk_bf16_f32 v172, v88, v89\n"
    "v_cvt_pk_bf16_f32 v173, v90, v91\n"
    "v_pk_mul_f32 v[92:93], v[92:93], v[242:243]\n"
    "v_pk_mul_f32 v[94:95], v[94:95], v[242:243]\n"
    "v_cvt_pk_bf16_f32 v174, v92, v93\n"
    "v_cvt_pk_bf16_f32 v175, v94, v95\n"
    "s_waitcnt lgkmcnt(0)\n"
    "s_add_u32 s67, s54, 4096\n"
    "buffer_store_dwordx4 v[16:19], v240, s[28:31], s67 offen offset:0 nt\n"
    "buffer_store_dwordx4 v[20:23], v240, s[28:31], s67 offen offset:1024 nt\n"
    "buffer_store_dwordx4 v[24:27], v240, s[28:31], s67 offen offset:2048 nt\n"
    "buffer_store_dwordx4 v[28:31], v240, s[28:31], s67 offen offset:3072 nt\n"
    "ds_write_b64 v235, v[160:161] offset:8704\n"
    "ds_write_b64 v235, v[162:163] offset:8736\n"
    "ds_write_b64 v235, v[164:165] offset:8768\n"
    "ds_write_b64 v235, v[166:167] offset:8800\n"
    "ds_write_b64 v235, v[168:169] offset:8832\n"
    "ds_write_b64 v235, v[170:171] offset:8864\n"
    "ds_write_b64 v235, v[172:173] offset:8896\n"
    "ds_write_b64 v235, v[174:175] offset:8928\n"
    "v_rcp_f32 v242, v231\n"
    "s_nop 0\n"
    "v_mov_b32 v243, v242\n"
    "v_pk_mul_f32 v[96:97], v[96:97], v[242:243]\n"
    "v_pk_mul_f32 v[98:99], v[98:99], v[242:243]\n"
    "v_cvt_pk_bf16_f32 v128, v96, v97\n"
    "v_cvt_pk_bf16_f32 v129, v98, v99\n"
    "v_pk_mul_f32 v[100:101], v[100:101], v[242:243]\n"
    "v_pk_mul_f32 v[102:103], v[102:103], v[242:243]\n"
    "v_cvt_pk_bf16_f32 v130, v100, v101\n"
    "v_cvt_pk_bf16_f32 v131, v102, v103\n"
    "s_waitcnt lgkmcnt(0)\n"
    "s_add_u32 s62, s81, 8704\n"
    "s_add_u32 s62, s62, s71\n"
    "v_add_u32 v244, s62, v239\n"
    "ds_read_b128 v[32:35], v244 offset:0\n"
    "ds_read_b128 v[36:39], v244 offset:1088\n"
    "ds_read_b128 v[40:43], v244 offset:2176\n"
    "ds_read_b128 v[44:47], v244 offset:3264\n"
    "v_pk_mul_f32 v[104:105], v[104:105], v[242:243]\n"
    "v_pk_mul_f32 v[106:107], v[106:107], v[242:243]\n"
    "v_cvt_pk_bf16_f32 v132, v104, v105\n"
    "v_cvt_pk_bf16_f32 v133, v106, v107\n"
    "v_pk_mul_f32 v[108:109], v[108:109], v[242:243]\n"
    "v_pk_mul_f32 v[110:111], v[110:111], v[242:243]\n"
    "v_cvt_pk_bf16_f32 v134, v108, v109\n"
    "v_cvt_pk_bf16_f32 v135, v110, v111\n"
    "v_pk_mul_f32 v[112:113], v[112:113], v[242:243]\n"
    "v_pk_mul_f32 v[114:115], v[114:115], v[242:243]\n"
    "v_cvt_pk_bf16_f32 v136, v112, v113\n"
    "v_cvt_pk_bf16_f32 v137, v114, v115\n"
    "v_pk_mul_f32 v[116:117], v[116:117], v[242:243]\n"
    "v_pk_mul_f32 v[118:119], v[118:119], v[242:243]\n"
    "v_cvt_pk_bf16_f32 v138, v116, v117\n"
    "v_cvt_pk_bf16_f32 v139, v118, v119\n"
    "v_pk_mul_f32 v[120:121], v[120:121], v[242:243]\n"
    "v_pk_mul_f32 v[122:123], v[122:123], v[242:243]\n"
    "v_cvt_pk_bf16_f32 v140, v120, v121\n"
    "v_cvt_pk_bf16_f32 v141, v122, v123\n"
    "v_pk_mul_f32 v[124:125], v[124:125], v[242:243]\n"
    "v_pk_mul_f32 v[126:127], v[126:127], v[242:243]\n"
    "v_cvt_pk_bf16_f32 v142, v124, v125\n"
    "v_cvt_pk_bf16_f32 v143, v126, v127\n"
    "s_waitcnt lgkmcnt(0)\n"
    "s_add_u32 s67, s54, 8192\n"
    "buffer_store_dwordx4 v[32:35], v240, s[28:31], s67 offen offset:0 nt\n"
    "buffer_store_dwordx4 v[36:39], v240, s[28:31], s67 offen offset:1024 nt\n"
    "buffer_store_dwordx4 v[40:43], v240, s[28:31], s67 offen offset:2048 nt\n"
    "buffer_store_dwordx4 v[44:47], v240, s[28:31], s67 offen offset:3072 nt\n"
    "ds_write_b64 v235, v[128:129] offset:13056\n"
    "ds_write_b64 v235, v[130:131] offset:13088\n"
    "ds_write_b64 v235, v[132:133] offset:13120\n"
    "ds_write_b64 v235, v[134:135] offset:13152\n"
    "ds_write_b64 v235, v[136:137] offset:13184\n"
    "ds_write_b64 v235, v[138:139] offset:13216\n"
    "ds_write_b64 v235, v[140:141] offset:13248\n"
    "ds_write_b64 v235, v[142:143] offset:13280\n"
    "v_rcp_f32 v242, v232\n"
    "s_nop 0\n"
    "v_mov_b32 v243, v242\n"
    "v_accvgpr_read_b32 v176, a0\n"
    "v_accvgpr_read_b32 v177, a1\n"
    "v_accvgpr_read_b32 v178, a2\n"
    "v_accvgpr_read_b32 v179, a3\n"
    "v_pk_mul_f32 v[176:177], v[176:177], v[242:243]\n"
    "v_pk_mul_f32 v[178:179], v[178:179], v[242:243]\n"
    "v_cvt_pk_bf16_f32 v144, v176, v177\n"
    "v_cvt_pk_bf16_f32 v145, v178, v179\n"
    "v_accvgpr_read_b32 v184, a4\n"
    "v_accvgpr_read_b32 v185, a5\n"
    "v_accvgpr_read_b32 v186, a6\n"
    "v_accvgpr_read_b32 v187, a7\n"
    "v_pk_mul_f32 v[184:185], v[184:185], v[242:243]\n"
    "v_pk_mul_f32 v[186:187], v[186:187], v[242:243]\n"
    "v_cvt_pk_bf16_f32 v146, v184, v185\n"
    "v_cvt_pk_bf16_f32 v147, v186, v187\n"
    "v_accvgpr_read_b32 v176, a8\n"
    "v_accvgpr_read_b32 v177, a9\n"
    "v_accvgpr_read_b32 v178, a10\n"
    "s_waitcnt lgkmcnt(0)\n"
    "s_add_u32 s62, s81, 13056\n"
    "s_add_u32 s62, s62, s71\n"
    "v_add_u32 v244, s62, v239\n"
    "ds_read_b128 v[0:3], v244 offset:0\n"
    "ds_read_b128 v[4:7], v244 offset:1088\n"
    "ds_read_b128 v[8:11], v244 offset:2176\n"
    "ds_read_b128 v[12:15], v244 offset:3264\n"
    "v_accvgpr_read_b32 v179, a11\n"
    "v_pk_mul_f32 v[176:177], v[176:177], v[242:243]\n"
    "v_pk_mul_f32 v[178:179], v[178:179], v[242:243]\n"
    "v_cvt_pk_bf16_f32 v148, v176, v177\n"
    "v_cvt_pk_bf16_f32 v149, v178, v179\n"
    "v_accvgpr_read_b32 v184, a12\n"
    "v_accvgpr_read_b32 v185, a13\n"
    "v_accvgpr_read_b32 v186, a14\n"
    "v_accvgpr_read_b32 v187, a15\n"
    "v_pk_mul_f32 v[184:185], v[184:185], v[242:243]\n"
    "v_pk_mul_f32 v[186:187], v[186:187], v[242:243]\n"
    "v_cvt_pk_bf16_f32 v150, v184, v185\n"
    "v_cvt_pk_bf16_f32 v151, v186, v187\n"
    "v_accvgpr_read_b32 v176, a16\n"
    "v_accvgpr_read_b32 v177, a17\n"
    "v_accvgpr_read_b32 v178, a18\n"
    "v_accvgpr_read_b32 v179, a19\n"
    "v_pk_mul_f32 v[176:177], v[176:177], v[242:243]\n"
    "v_pk_mul_f32 v[178:179], v[178:179], v[242:243]\n"
    "v_cvt_pk_bf16_f32 v152, v176, v177\n"
    "v_cvt_pk_bf16_f32 v153, v178, v179\n"
    "v_accvgpr_read_b32 v184, a20\n"
    "v_accvgpr_read_b32 v185, a21\n"
    "v_accvgpr_read_b32 v186, a22\n"
    "v_accvgpr_read_b32 v187, a23\n"
    "v_pk_mul_f32 v[184:185], v[184:185], v[242:243]\n"
    "v_pk_mul_f32 v[186:187], v[186:187], v[242:243]\n"
    "v_cvt_pk_bf16_f32 v154, v184, v185\n"
    "v_cvt_pk_bf16_f32 v155, v186, v187\n"
    "v_accvgpr_read_b32 v176, a24\n"
    "v_accvgpr_read_b32 v177, a25\n"
    "v_accvgpr_read_b32 v178, a26\n"
    "v_accvgpr_read_b32 v179, a27\n"
    "v_pk_mul_f32 v[176:177], v[176:177], v[242:243]\n"
    "v_pk_mul_f32 v[178:179], v[178:179], v[242:243]\n"
    "v_cvt_pk_bf16_f32 v156, v176, v177\n"
    "v_cvt_pk_bf16_f32 v157, v178, v179\n"
    "v_accvgpr_read_b32 v184, a28\n"
    "v_accvgpr_read_b32 v185, a29\n"
    "v_accvgpr_read_b32 v186, a30\n"
    "v_accvgpr_read_b32 v187, a31\n"
    "v_pk_mul_f32 v[184:185], v[184:185], v[242:243]\n"
    "v_pk_mul_f32 v[186:187], v[186:187], v[242:243]\n"
    "v_cvt_pk_bf16_f32 v158, v184, v185\n"
    "v_cvt_pk_bf16_f32 v159, v186, v187\n"
    "s_waitcnt lgkmcnt(0)\n"
    "s_add_u32 s67, s54, 12288\n"
    "buffer_store_dwordx4 v[0:3], v240, s[28:31], s67 offen offset:0 nt\n"
    "buffer_store_dwordx4 v[4:7], v240, s[28:31], s67 offen offset:1024 nt\n"
    "buffer_store_dwordx4 v[8:11], v240, s[28:31], s67 offen offset:2048 nt\n"
    "buffer_store_dwordx4 v[12:15], v240, s[28:31], s67 offen offset:3072 nt\n"
    "ds_write_b64 v235, v[144:145] offset:17408\n"
    "ds_write_b64 v235, v[146:147] offset:17440\n"
    "ds_write_b64 v235, v[148:149] offset:17472\n"
    "ds_write_b64 v235, v[150:151] offset:17504\n"
    "ds_write_b64 v235, v[152:153] offset:17536\n"
    "ds_write_b64 v235, v[154:155] offset:17568\n"
    "ds_write_b64 v235, v[156:157] offset:17600\n"
    "ds_write_b64 v235, v[158:159] offset:17632\n"
    "s_waitcnt lgkmcnt(0)\n"
    "s_add_u32 s62, s81, 17408\n"
    "s_add_u32 s62, s62, s71\n"
    "v_add_u32 v244, s62, v239\n"
    "ds_read_b128 v[16:19], v244 offset:0\n"
    "ds_read_b128 v[20:23], v244 offset:1088\n"
    "ds_read_b128 v[24:27], v244 offset:2176\n"
    "ds_read_b128 v[28:31], v244 offset:3264\n"
    "s_waitcnt lgkmcnt(0)\n"
    "s_add_u32 s67, s54, 16384\n"
    "buffer_store_dwordx4 v[16:19], v240, s[28:31], s67 offen offset:0 nt\n"
    "buffer_store_dwordx4 v[20:23], v240, s[28:31], s67 offen offset:1024 nt\n"
    "buffer_store_dwordx4 v[24:27], v240, s[28:31], s67 offen offset:2048 nt\n"
    "buffer_store_dwordx4 v[28:31], v240, s[28:31], s67 offen offset:3072 nt\n"
    "s_waitcnt vmcnt(0)\n"
    "s_branch END_%=\n"
    "RARE0_%=:\n"
    "s_nop 7\n"
    "s_nop 7\n"
    "s_cselect_b32 s65, 1, 0\n"
    "v_max3_f32 v242, v128, v129, v130\n"
    "v_max3_f32 v242, v242, v131, v132\n"
    "v_max3_f32 v242, v242, v133, v134\n"
    "v_max3_f32 v242, v242, v135, v136\n"
    "v_max3_f32 v242, v242, v137, v138\n"
    "v_max3_f32 v242, v242, v139, v140\n"
    "v_max3_f32 v242, v242, v141, v142\n"
    "v_max_f32 v242, v242, v143\n"
    "v_mov_b32 v243, v242\n"
    "s_nop 1\n"
    "v_permlane16_swap_b32 v242, v243\n"
    "s_nop 1\n"
    "v_max_f32 v242, v242, v243\n"
    "v_mov_b32 v243, v242\n"
    "s_nop 1\n"
    "v_permlane32_swap_b32 v242, v243\n"
    "s_nop 1\n"
    "v_max_f32 v242, v242, v243\n"
    "v_max_f32 v242, 0, v242\n"
    "v_exp_f32 v243, -v242\n"
    "s_nop 1\n"
    "v_sub_f32 v200, v200, v242\n"
    "v_sub_f32 v201, v201, v242\n"
    "v_sub_f32 v202, v202, v242\n"
    "v_sub_f32 v203, v203, v242\n"
    "v_mul_f32 v228, v228, v243\n"
    "v_mul_f32 v0, v0, v243\n"
    "v_mul_f32 v1, v1, v243\n"
    "v_mul_f32 v2, v2, v243\n"
    "v_mul_f32 v3, v3, v243\n"
    "v_mul_f32 v4, v4, v243\n"
    "v_mul_f32 v5, v5, v243\n"
    "v_mul_f32 v6, v6, v243\n"
    "v_mul_f32 v7, v7, v243\n"
    "v_mul_f32 v8, v8, v243\n"
    "v_mul_f32 v9, v9, v243\n"
    "v_mul_f32 v10, v10, v243\n"
    "v_mul_f32 v11, v11, v243\n"
    "v_mul_f32 v12, v12, v243\n"
    "v_mul_f32 v13, v13, v243\n"
    "v_mul_f32 v14, v14, v243\n"
    "v_mul_f32 v15, v15, v243\n"
    "v_mul_f32 v16, v16, v243\n"
    "v_mul_f32 v17, v17, v243\n"
    "v_mul_f32 v18, v18, v243\n"
    "v_mul_f32 v19, v19, v243\n"
    "v_mul_f32 v20, v20, v243\n"
    "v_mul_f32 v21, v21, v243\n"
    "v_mul_f32 v22, v22, v243\n"
    "v_mul_f32 v23, v23, v243\n"
    "v_mul_f32 v24, v24, v243\n"
    "v_mul_f32 v25, v25, v243\n"
    "v_mul_f32 v26, v26, v243\n"
    "v_mul_f32 v27, v27, v243\n"
    "v_mul_f32 v28, v28, v243\n"
    "v_mul_f32 v29, v29, v243\n"
    "v_mul_f32 v30, v30, v243\n"
    "v_mul_f32 v31, v31, v243\n"
    "v_sub_f32 v244, v128, v242\n"
    "v_sub_f32 v245, v129, v242\n"
    "v_exp_f32 v244, v244\n"
    "v_exp_f32 v245, v245\n"
    "s_nop 0\n"
    "v_cvt_pk_bf16_f32 v176, v244, v245\n"
    "v_sub_f32 v244, v130, v242\n"
    "v_sub_f32 v245, v131, v242\n"
    "v_exp_f32 v244, v244\n"
    "v_exp_f32 v245, v245\n"
    "s_nop 0\n"
    "v_cvt_pk_bf16_f32 v177, v244, v245\n"
    "v_sub_f32 v244, v132, v242\n"
    "v_sub_f32 v245, v133, v242\n"
    "v_exp_f32 v244, v244\n"
    "v_exp_f32 v245, v245\n"
    "s_nop 0\n"
    "v_cvt_pk_bf16_f32 v178, v244, v245\n"
    "v_sub_f32 v244, v134, v242\n"
    "v_sub_f32 v245, v135, v242\n"
    "v_exp_f32 v244, v244\n"
    "v_exp_f32 v245, v245\n"
    "s_nop 0\n"
    "v_cvt_pk_bf16_f32 v179, v244, v245\n"
    "v_sub_f32 v244, v136, v242\n"
    "v_sub_f32 v245, v137, v242\n"
    "v_exp_f32 v244, v244\n"
    "v_exp_f32 v245, v245\n"
    "s_nop 0\n"
    "v_cvt_pk_bf16_f32 v180, v244, v245\n"
    "v_sub_f32 v244, v138, v242\n"
    "v_sub_f32 v245, v139, v242\n"
    "v_exp_f32 v244, v244\n"
    "v_exp_f32 v245, v245\n"
    "s_nop 0\n"
    "v_cvt_pk_bf16_f32 v181, v244, v245\n"
    "v_sub_f32 v244, v140, v242\n"
    "v_sub_f32 v245, v141, v242\n"
    "v_exp_f32 v244, v244\n"
    "v_exp_f32 v245, v245\n"
    "s_nop 0\n"
    "v_cvt_pk_bf16_f32 v182, v244, v245\n"
    "v_sub_f32 v244, v142, v242\n"
    "v_sub_f32 v245, v143, v242\n"
    "v_exp_f32 v244, v244\n"
    "v_exp_f32 v245, v245\n"
    "s_nop 0\n"
    "v_cvt_pk_bf16_f32 v183, v244, v245\n"
    "s_nop 1\n"
    "v_mfma_f32_16x16x32_bf16 v[220:223], a[240:243], v[176:179], 0\n"
    "v_mfma_f32_16x16x32_bf16 v[220:223], a[240:243], v[180:183], v[220:223]\n"
    "s_nop 7\n"
    "s_nop 3\n"
    "s_cmp_eq_u32 s40, 3\n"
    "s_cbranch_scc1 RARE0W_%=\n"
    "s_cmp_lg_u32 s65, 0\n"
    "s_branch RET0_%=\n"
    "RARE0W_%=:\n"
    "s_cmp_lg_u32 s65, 0\n"
    "s_branch RET0W_%=\n"
    "RARE1_%=:\n"
    "s_nop 7\n"
    "s_nop 7\n"
    "s_cselect_b32 s65, 1, 0\n"
    "v_max3_f32 v242, v144, v145, v146\n"
    "v_max3_f32 v242, v242, v147, v148\n"
    "v_max3_f32 v242, v242, v149, v150\n"
    "v_max3_f32 v242, v242, v151, v152\n"
    "v_max3_f32 v242, v242, v153, v154\n"
    "v_max3_f32 v242, v242, v155, v156\n"
    "v_max3_f32 v242, v242, v157, v158\n"
    "v_max_f32 v242, v242, v159\n"
    "v_mov_b32 v243, v242\n"
    "s_nop 1\n"
    "v_permlane16_swap_b32 v242, v243\n"
    "s_nop 1\n"
    "v_max_f32 v242, v242, v243\n"
    "v_mov_b32 v243, v242\n"
    "s_nop 1\n"
    "v_permlane32_swap_b32 v242, v243\n"
    "s_nop 1\n"
    "v_max_f32 v242, v242, v243\n"
    "v_max_f32 v242, 0, v242\n"
    "v_exp_f32 v243, -v242\n"
    "s_nop 1\n"
    "v_sub_f32 v204, v204, v242\n"
    "v_sub_f32 v205, v205, v242\n"
    "v_sub_f32 v206, v206, v242\n"
    "v_sub_f32 v207, v207, v242\n"
    "v_mul_f32 v229, v229, v243\n"
    "v_mul_f32 v32, v32, v243\n"
    "v_mul_f32 v33, v33, v243\n"
    "v_mul_f32 v34, v34, v243\n"
    "v_mul_f32 v35, v35, v243\n"
    "v_mul_f32 v36, v36, v243\n"
    "v_mul_f32 v37, v37, v243\n"
    "v_mul_f32 v38, v38, v243\n"
    "v_mul_f32 v39, v39, v243\n"
    "v_mul_f32 v40, v40, v243\n"
    "v_mul_f32 v41, v41, v243\n"
    "v_mul_f32 v42, v42, v243\n"
    "v_mul_f32 v43, v43, v243\n"
    "v_mul_f32 v44, v44, v243\n"
    "v_mul_f32 v45, v45, v243\n"
    "v_mul_f32 v46, v46, v243\n"
    "v_mul_f32 v47, v47, v243\n"
    "v_mul_f32 v48, v48, v243\n"
    "v_mul_f32 v49, v49, v243\n"
    "v_mul_f32 v50, v50, v243\n"
    "v_mul_f32 v51, v51, v243\n"
    "v_mul_f32 v52, v52, v243\n"
    "v_mul_f32 v53, v53, v243\n"
    "v_mul_f32 v54, v54, v243\n"
    "v_mul_f32 v55, v55, v243\n"
    "v_mul_f32 v56, v56, v243\n"
    "v_mul_f32 v57, v57, v243\n"
    "v_mul_f32 v58, v58, v243\n"
    "v_mul_f32 v59, v59, v243\n"
    "v_mul_f32 v60, v60, v243\n"
    "v_mul_f32 v61, v61, v243\n"
    "v_mul_f32 v62, v62, v243\n"
    "v_mul_f32 v63, v63, v243\n"
    "v_sub_f32 v244, v144, v242\n"
    "v_sub_f32 v245, v145, v242\n"
    "v_exp_f32 v244, v244\n"
    "v_exp_f32 v245, v245\n"
    "s_nop 0\n"
    "v_cvt_pk_bf16_f32 v184, v244, v245\n"
    "v_sub_f32 v244, v146, v242\n"
    "v_sub_f32 v245, v147, v242\n"
    "v_exp_f32 v244, v244\n"
    "v_exp_f32 v245, v245\n"
    "s_nop 0\n"
    "v_cvt_pk_bf16_f32 v185, v244, v245\n"
    "v_sub_f32 v244, v148, v242\n"
    "v_sub_f32 v245, v149, v242\n"
    "v_exp_f32 v244, v244\n"
    "v_exp_f32 v245, v245\n"
    "s_nop 0\n"
    "v_cvt_pk_bf16_f32 v186, v244, v245\n"
    "v_sub_f32 v244, v150, v242\n"
    "v_sub_f32 v245, v151, v242\n"
    "v_exp_f32 v244, v244\n"
    "v_exp_f32 v245, v245\n"
    "s_nop 0\n"
    "v_cvt_pk_bf16_f32 v187, v244, v245\n"
    "v_sub_f32 v244, v152, v242\n"
    "v_sub_f32 v245, v153, v242\n"
    "v_exp_f32 v244, v244\n"
    "v_exp_f32 v245, v245\n"
    "s_nop 0\n"
    "v_cvt_pk_bf16_f32 v188, v244, v245\n"
    "v_sub_f32 v244, v154, v242\n"
    "v_sub_f32 v245, v155, v242\n"
    "v_exp_f32 v244, v244\n"
    "v_exp_f32 v245, v245\n"
    "s_nop 0\n"
    "v_cvt_pk_bf16_f32 v189, v244, v245\n"
    "v_sub_f32 v244, v156, v242\n"
    "v_sub_f32 v245, v157, v242\n"
    "v_exp_f32 v244, v244\n"
    "v_exp_f32 v245, v245\n"
    "s_nop 0\n"
    "v_cvt_pk_bf16_f32 v190, v244, v245\n"
    "v_sub_f32 v244, v158, v242\n"
    "v_sub_f32 v245, v159, v242\n"
    "v_exp_f32 v244, v244\n"
    "v_exp_f32 v245, v245\n"
    "s_nop 0\n"
    "v_cvt_pk_bf16_f32 v191, v244, v245\n"
    "s_nop 1\n"
    "v_mfma_f32_16x16x32_bf16 v[220:223], a[240:243], v[184:187], 0\n"
    "v_mfma_f32_16x16x32_bf16 v[220:223], a[240:243], v[188:191], v[220:223]\n"
    "s_nop 7\n"
    "s_nop 3\n"
    "s_cmp_eq_u32 s40, 3\n"
    "s_cbranch_scc1 RARE1W_%=\n"
    "s_cmp_lg_u32 s65, 0\n"
    "s_branch RET1_%=\n"
    "RARE1W_%=:\n"
    "s_cmp_lg_u32 s65, 0\n"
    "s_branch RET1W_%=\n"
    "RARE2_%=:\n"
    "s_nop 7\n"
    "s_nop 7\n"
    "s_cselect_b32 s65, 1, 0\n"
    "v_max3_f32 v242, v128, v129, v130\n"
    "v_max3_f32 v242, v242, v131, v132\n"
    "v_max3_f32 v242, v242, v133, v134\n"
    "v_max3_f32 v242, v242, v135, v136\n"
    "v_max3_f32 v242, v242, v137, v138\n"
    "v_max3_f32 v242, v242, v139, v140\n"
    "v_max3_f32 v242, v242, v141, v142\n"
    "v_max_f32 v242, v242, v143\n"
    "v_mov_b32 v243, v242\n"
    "s_nop 1\n"
    "v_permlane16_swap_b32 v242, v243\n"
    "s_nop 1\n"
    "v_max_f32 v242, v242, v243\n"
    "v_mov_b32 v243, v242\n"
    "s_nop 1\n"
    "v_permlane32_swap_b32 v242, v243\n"
    "s_nop 1\n"
    "v_max_f32 v242, v242, v243\n"
    "v_max_f32 v242, 0, v242\n"
    "v_exp_f32 v243, -v242\n"
    "s_nop 1\n"
    "v_sub_f32 v208, v208, v242\n"
    "v_sub_f32 v209, v209, v242\n"
    "v_sub_f32 v210, v210, v242\n"
    "v_sub_f32 v211, v211, v242\n"
    "v_mul_f32 v230, v230, v243\n"
    "v_mul_f32 v64, v64, v243\n"
    "v_mul_f32 v65, v65, v243\n"
    "v_mul_f32 v66, v66, v243\n"
    "v_mul_f32 v67, v67, v243\n"
    "v_mul_f32 v68, v68, v243\n"
    "v_mul_f32 v69, v69, v243\n"
    "v_mul_f32 v70, v70, v243\n"
    "v_mul_f32 v71, v71, v243\n"
    "v_mul_f32 v72, v72, v243\n"
    "v_mul_f32 v73, v73, v243\n"
    "v_mul_f32 v74, v74, v243\n"
    "v_mul_f32 v75, v75, v243\n"
    "v_mul_f32 v76, v76, v243\n"
    "v_mul_f32 v77, v77, v243\n"
    "v_mul_f32 v78, v78, v243\n"
    "v_mul_f32 v79, v79, v243\n"
    "v_mul_f32 v80, v80, v243\n"
    "v_mul_f32 v81, v81, v243\n"
    "v_mul_f32 v82, v82, v243\n"
    "v_mul_f32 v83, v83, v243\n"
    "v_mul_f32 v84, v84, v243\n"
    "v_mul_f32 v85, v85, v243\n"
    "v_mul_f32 v86, v86, v243\n"
    "v_mul_f32 v87, v87, v243\n"
    "v_mul_f32 v88, v88, v243\n"
    "v_mul_f32 v89, v89, v243\n"
    "v_mul_f32 v90, v90, v243\n"
    "v_mul_f32 v91, v91, v243\n"
    "v_mul_f32 v92, v92, v243\n"
    "v_mul_f32 v93, v93, v243\n"
    "v_mul_f32 v94, v94, v243\n"
    "v_mul_f32 v95, v95, v243\n"
    "v_sub_f32 v244, v128, v242\n"
    "v_sub_f32 v245, v129, v242\n"
    "v_exp_f32 v244, v244\n"
    "v_exp_f32 v245, v245\n"
    "s_nop 0\n"
    "v_cvt_pk_bf16_f32 v176, v244, v245\n"
    "v_sub_f32 v244, v130, v242\n"
    "v_sub_f32 v245, v131, v242\n"
    "v_exp_f32 v244, v244\n"
    "v_exp_f32 v245, v245\n"
    "s_nop 0\n"
    "v_cvt_pk_bf16_f32 v177, v244, v245\n"
    "v_sub_f32 v244, v132, v242\n"
    "v_sub_f32 v245, v133, v242\n"
    "v_exp_f32 v244, v244\n"
    "v_exp_f32 v245, v245\n"
    "s_nop 0\n"
    "v_cvt_pk_bf16_f32 v178, v244, v245\n"
    "v_sub_f32 v244, v134, v242\n"
    "v_sub_f32 v245, v135, v242\n"
    "v_exp_f32 v244, v244\n"
    "v_exp_f32 v245, v245\n"
    "s_nop 0\n"
    "v_cvt_pk_bf16_f32 v179, v244, v245\n"
    "v_sub_f32 v244, v136, v242\n"
    "v_sub_f32 v245, v137, v242\n"
    "v_exp_f32 v244, v244\n"
    "v_exp_f32 v245, v245\n"
    "s_nop 0\n"
    "v_cvt_pk_bf16_f32 v180, v244, v245\n"
    "v_sub_f32 v244, v138, v242\n"
    "v_sub_f32 v245, v139, v242\n"
    "v_exp_f32 v244, v244\n"
    "v_exp_f32 v245, v245\n"
    "s_nop 0\n"
    "v_cvt_pk_bf16_f32 v181, v244, v245\n"
    "v_sub_f32 v244, v140, v242\n"
    "v_sub_f32 v245, v141, v242\n"
    "v_exp_f32 v244, v244\n"
    "v_exp_f32 v245, v245\n"
    "s_nop 0\n"
    "v_cvt_pk_bf16_f32 v182, v244, v245\n"
    "v_sub_f32 v244, v142, v242\n"
    "v_sub_f32 v245, v143, v242\n"
    "v_exp_f32 v244, v244\n"
    "v_exp_f32 v245, v245\n"
    "s_nop 0\n"
    "v_cvt_pk_bf16_f32 v183, v244, v245\n"
    "s_nop 1\n"
    "v_mfma_f32_16x16x32_bf16 v[220:223], a[240:243], v[176:179], 0\n"
    "v_mfma_f32_16x16x32_bf16 v[220:223], a[240:243], v[180:183], v[220:223]\n"
    "s_nop 7\n"
    "s_nop 3\n"
    "s_cmp_eq_u32 s40, 3\n"
    "s_cbranch_scc1 RARE2W_%=\n"
    "s_cmp_lg_u32 s65, 0\n"
    "s_branch RET2_%=\n"
    "RARE2W_%=:\n"
    "s_cmp_lg_u32 s65, 0\n"
    "s_branch RET2W_%=\n"
    "RARE3_%=:\n"
    "s_nop 7\n"
    "s_nop 7\n"
    "s_cselect_b32 s65, 1, 0\n"
    "v_max3_f32 v242, v144, v145, v146\n"
    "v_max3_f32 v242, v242, v147, v148\n"
    "v_max3_f32 v242, v242, v149, v150\n"
    "v_max3_f32 v242, v242, v151, v152\n"
    "v_max3_f32 v242, v242, v153, v154\n"
    "v_max3_f32 v242, v242, v155, v156\n"
    "v_max3_f32 v242, v242, v157, v158\n"
    "v_max_f32 v242, v242, v159\n"
    "v_mov_b32 v243, v242\n"
    "s_nop 1\n"
    "v_permlane16_swap_b32 v242, v243\n"
    "s_nop 1\n"
    "v_max_f32 v242, v242, v243\n"
    "v_mov_b32 v243, v242\n"
    "s_nop 1\n"
    "v_permlane32_swap_b32 v242, v243\n"
    "s_nop 1\n"
    "v_max_f32 v242, v242, v243\n"
    "v_max_f32 v242, 0, v242\n"
    "v_exp_f32 v243, -v242\n"
    "s_nop 1\n"
    "v_sub_f32 v212, v212, v242\n"
    "v_sub_f32 v213, v213, v242\n"
    "v_sub_f32 v214, v214, v242\n"
    "v_sub_f32 v215, v215, v242\n"
    "v_mul_f32 v231, v231, v243\n"
    "v_mul_f32 v96, v96, v243\n"
    "v_mul_f32 v97, v97, v243\n"
    "v_mul_f32 v98, v98, v243\n"
    "v_mul_f32 v99, v99, v243\n"
    "v_mul_f32 v100, v100, v243\n"
    "v_mul_f32 v101, v101, v243\n"
    "v_mul_f32 v102, v102, v243\n"
    "v_mul_f32 v103, v103, v243\n"
    "v_mul_f32 v104, v104, v243\n"
    "v_mul_f32 v105, v105, v243\n"
    "v_mul_f32 v106, v106, v243\n"
    "v_mul_f32 v107, v107, v243\n"
    "v_mul_f32 v108, v108, v243\n"
    "v_mul_f32 v109, v109, v243\n"
    "v_mul_f32 v110, v110, v243\n"
    "v_mul_f32 v111, v111, v243\n"
    "v_mul_f32 v112, v112, v243\n"
    "v_mul_f32 v113, v113, v243\n"
    "v_mul_f32 v114, v114, v243\n"
    "v_mul_f32 v115, v115, v243\n"
    "v_mul_f32 v116, v116, v243\n"
    "v_mul_f32 v117, v117, v243\n"
    "v_mul_f32 v118, v118, v243\n"
    "v_mul_f32 v119, v119, v243\n"
    "v_mul_f32 v120, v120, v243\n"
    "v_mul_f32 v121, v121, v243\n"
    "v_mul_f32 v122, v122, v243\n"
    "v_mul_f32 v123, v123, v243\n"
    "v_mul_f32 v124, v124, v243\n"
    "v_mul_f32 v125, v125, v243\n"
    "v_mul_f32 v126, v126, v243\n"
    "v_mul_f32 v127, v127, v243\n"
    "v_sub_f32 v244, v144, v242\n"
    "v_sub_f32 v245, v145, v242\n"
    "v_exp_f32 v244, v244\n"
    "v_exp_f32 v245, v245\n"
    "s_nop 0\n"
    "v_cvt_pk_bf16_f32 v184, v244, v245\n"
    "v_sub_f32 v244, v146, v242\n"
    "v_sub_f32 v245, v147, v242\n"
    "v_exp_f32 v244, v244\n"
    "v_exp_f32 v245, v245\n"
    "s_nop 0\n"
    "v_cvt_pk_bf16_f32 v185, v244, v245\n"
    "v_sub_f32 v244, v148, v242\n"
    "v_sub_f32 v245, v149, v242\n"
    "v_exp_f32 v244, v244\n"
    "v_exp_f32 v245, v245\n"
    "s_nop 0\n"
    "v_cvt_pk_bf16_f32 v186, v244, v245\n"
    "v_sub_f32 v244, v150, v242\n"
    "v_sub_f32 v245, v151, v242\n"
    "v_exp_f32 v244, v244\n"
    "v_exp_f32 v245, v245\n"
    "s_nop 0\n"
    "v_cvt_pk_bf16_f32 v187, v244, v245\n"
    "v_sub_f32 v244, v152, v242\n"
    "v_sub_f32 v245, v153, v242\n"
    "v_exp_f32 v244, v244\n"
    "v_exp_f32 v245, v245\n"
    "s_nop 0\n"
    "v_cvt_pk_bf16_f32 v188, v244, v245\n"
    "v_sub_f32 v244, v154, v242\n"
    "v_sub_f32 v245, v155, v242\n"
    "v_exp_f32 v244, v244\n"
    "v_exp_f32 v245, v245\n"
    "s_nop 0\n"
    "v_cvt_pk_bf16_f32 v189, v244, v245\n"
    "v_sub_f32 v244, v156, v242\n"
    "v_sub_f32 v245, v157, v242\n"
    "v_exp_f32 v244, v244\n"
    "v_exp_f32 v245, v245\n"
    "s_nop 0\n"
    "v_cvt_pk_bf16_f32 v190, v244, v245\n"
    "v_sub_f32 v244, v158, v242\n"
    "v_sub_f32 v245, v159, v242\n"
    "v_exp_f32 v244, v244\n"
    "v_exp_f32 v245, v245\n"
    "s_nop 0\n"
    "v_cvt_pk_bf16_f32 v191, v244, v245\n"
    "s_nop 1\n"
    "v_mfma_f32_16x16x32_bf16 v[220:223], a[240:243], v[184:187], 0\n"
    "v_mfma_f32_16x16x32_bf16 v[220:223], a[240:243], v[188:191], v[220:223]\n"
    "s_nop 7\n"
    "s_nop 3\n"
    "s_cmp_eq_u32 s40, 3\n"
    "s_cbranch_scc1 RARE3W_%=\n"
    "s_cmp_lg_u32 s65, 0\n"
    "s_branch RET3_%=\n"
    "RARE3W_%=:\n"
    "s_cmp_lg_u32 s65, 0\n"
    "s_branch RET3W_%=\n"
    "RARE4_%=:\n"
    "s_nop 7\n"
    "s_nop 7\n"
    "s_cselect_b32 s65, 1, 0\n"
    "v_max3_f32 v242, v160, v161, v162\n"
    "v_max3_f32 v242, v242, v163, v164\n"
    "v_max3_f32 v242, v242, v165, v166\n"
    "v_max3_f32 v242, v242, v167, v168\n"
    "v_max3_f32 v242, v242, v169, v170\n"
    "v_max3_f32 v242, v242, v171, v172\n"
    "v_max3_f32 v242, v242, v173, v174\n"
    "v_max_f32 v242, v242, v175\n"
    "v_mov_b32 v243, v242\n"
    "s_nop 1\n"
    "v_permlane16_swap_b32 v242, v243\n"
    "s_nop 1\n"
    "v_max_f32 v242, v242, v243\n"
    "v_mov_b32 v243, v242\n"
    "s_nop 1\n"
    "v_permlane32_swap_b32 v242, v243\n"
    "s_nop 1\n"
    "v_max_f32 v242, v242, v243\n"
    "v_max_f32 v242, 0, v242\n"
    "v_exp_f32 v243, -v242\n"
    "s_nop 1\n"
    "v_sub_f32 v216, v216, v242\n"
    "v_sub_f32 v217, v217, v242\n"
    "v_sub_f32 v218, v218, v242\n"
    "v_sub_f32 v219, v219, v242\n"
    "v_mul_f32 v232, v232, v243\n"
    "v_accvgpr_read_b32 v244, a0\n"
    "v_mul_f32 v244, v244, v243\n"
    "v_accvgpr_write_b32 a0, v244\n"
    "v_accvgpr_read_b32 v244, a1\n"
    "v_mul_f32 v244, v244, v243\n"
    "v_accvgpr_write_b32 a1, v244\n"
    "v_accvgpr_read_b32 v244, a2\n"
    "v_mul_f32 v244, v244, v243\n"
    "v_accvgpr_write_b32 a2, v244\n"
    "v_accvgpr_read_b32 v244, a3\n"
    "v_mul_f32 v244, v244, v243\n"
    "v_accvgpr_write_b32 a3, v244\n"
    "v_accvgpr_read_b32 v244, a4\n"
    "v_mul_f32 v244, v244, v243\n"
    "v_accvgpr_write_b32 a4, v244\n"
    "v_accvgpr_read_b32 v244, a5\n"
    "v_mul_f32 v244, v244, v243\n"
    "v_accvgpr_write_b32 a5, v244\n"
    "v_accvgpr_read_b32 v244, a6\n"
    "v_mul_f32 v244, v244, v243\n"
    "v_accvgpr_write_b32 a6, v244\n"
    "v_accvgpr_read_b32 v244, a7\n"
    "v_mul_f32 v244, v244, v243\n"
    "v_accvgpr_write_b32 a7, v244\n"
    "v_accvgpr_read_b32 v244, a8\n"
    "v_mul_f32 v244, v244, v243\n"
    "v_accvgpr_write_b32 a8, v244\n"
    "v_accvgpr_read_b32 v244, a9\n"
    "v_mul_f32 v244, v244, v243\n"
    "v_accvgpr_write_b32 a9, v244\n"
    "v_accvgpr_read_b32 v244, a10\n"
    "v_mul_f32 v244, v244, v243\n"
    "v_accvgpr_write_b32 a10, v244\n"
    "v_accvgpr_read_b32 v244, a11\n"
    "v_mul_f32 v244, v244, v243\n"
    "v_accvgpr_write_b32 a11, v244\n"
    "v_accvgpr_read_b32 v244, a12\n"
    "v_mul_f32 v244, v244, v243\n"
    "v_accvgpr_write_b32 a12, v244\n"
    "v_accvgpr_read_b32 v244, a13\n"
    "v_mul_f32 v244, v244, v243\n"
    "v_accvgpr_write_b32 a13, v244\n"
    "v_accvgpr_read_b32 v244, a14\n"
    "v_mul_f32 v244, v244, v243\n"
    "v_accvgpr_write_b32 a14, v244\n"
    "v_accvgpr_read_b32 v244, a15\n"
    "v_mul_f32 v244, v244, v243\n"
    "v_accvgpr_write_b32 a15, v244\n"
    "v_accvgpr_read_b32 v244, a16\n"
    "v_mul_f32 v244, v244, v243\n"
    "v_accvgpr_write_b32 a16, v244\n"
    "v_accvgpr_read_b32 v244, a17\n"
    "v_mul_f32 v244, v244, v243\n"
    "v_accvgpr_write_b32 a17, v244\n"
    "v_accvgpr_read_b32 v244, a18\n"
    "v_mul_f32 v244, v244, v243\n"
    "v_accvgpr_write_b32 a18, v244\n"
    "v_accvgpr_read_b32 v244, a19\n"
    "v_mul_f32 v244, v244, v243\n"
    "v_accvgpr_write_b32 a19, v244\n"
    "v_accvgpr_read_b32 v244, a20\n"
    "v_mul_f32 v244, v244, v243\n"
    "v_accvgpr_write_b32 a20, v244\n"
    "v_accvgpr_read_b32 v244, a21\n"
    "v_mul_f32 v244, v244, v243\n"
    "v_accvgpr_write_b32 a21, v244\n"
    "v_accvgpr_read_b32 v244, a22\n"
    "v_mul_f32 v244, v244, v243\n"
    "v_accvgpr_write_b32 a22, v244\n"
    "v_accvgpr_read_b32 v244, a23\n"
    "v_mul_f32 v244, v244, v243\n"
    "v_accvgpr_write_b32 a23, v244\n"
    "v_accvgpr_read_b32 v244, a24\n"
    "v_mul_f32 v244, v244, v243\n"
    "v_accvgpr_write_b32 a24, v244\n"
    "v_accvgpr_read_b32 v244, a25\n"
    "v_mul_f32 v244, v244, v243\n"
    "v_accvgpr_write_b32 a25, v244\n"
    "v_accvgpr_read_b32 v244, a26\n"
    "v_mul_f32 v244, v244, v243\n"
    "v_accvgpr_write_b32 a26, v244\n"
    "v_accvgpr_read_b32 v244, a27\n"
    "v_mul_f32 v244, v244, v243\n"
    "v_accvgpr_write_b32 a27, v244\n"
    "v_accvgpr_read_b32 v244, a28\n"
    "v_mul_f32 v244, v244, v243\n"
    "v_accvgpr_write_b32 a28, v244\n"
    "v_accvgpr_read_b32 v244, a29\n"
    "v_mul_f32 v244, v244, v243\n"
    "v_accvgpr_write_b32 a29, v244\n"
    "v_accvgpr_read_b32 v244, a30\n"
    "v_mul_f32 v244, v244, v243\n"
    "v_accvgpr_write_b32 a30, v244\n"
    "v_accvgpr_read_b32 v244, a31\n"
    "v_mul_f32 v244, v244, v243\n"
    "v_accvgpr_write_b32 a31, v244\n"
    "v_sub_f32 v244, v160, v242\n"
    "v_sub_f32 v245, v161, v242\n"
    "v_exp_f32 v244, v244\n"
    "v_exp_f32 v245, v245\n"
    "s_nop 0\n"
    "v_cvt_pk_bf16_f32 v192, v244, v245\n"
    "v_sub_f32 v244, v162, v242\n"
    "v_sub_f32 v245, v163, v242\n"
    "v_exp_f32 v244, v244\n"
    "v_exp_f32 v245, v245\n"
    "s_nop 0\n"
    "v_cvt_pk_bf16_f32 v193, v244, v245\n"
    "v_sub_f32 v244, v164, v242\n"
    "v_sub_f32 v245, v165, v242\n"
    "v_exp_f32 v244, v244\n"
    "v_exp_f32 v245, v245\n"
    "s_nop 0\n"
    "v_cvt_pk_bf16_f32 v194, v244, v245\n"
    "v_sub_f32 v244, v166, v242\n"
    "v_sub_f32 v245, v167, v242\n"
    "v_exp_f32 v244, v244\n"
    "v_exp_f32 v245, v245\n"
    "s_nop 0\n"
    "v_cvt_pk_bf16_f32 v195, v244, v245\n"
    "v_sub_f32 v244, v168, v242\n"
    "v_sub_f32 v245, v169, v242\n"
    "v_exp_f32 v244, v244\n"
    "v_exp_f32 v245, v245\n"
    "s_nop 0\n"
    "v_cvt_pk_bf16_f32 v196, v244, v245\n"
    "v_sub_f32 v244, v170, v242\n"
    "v_sub_f32 v245, v171, v242\n"
    "v_exp_f32 v244, v244\n"
    "v_exp_f32 v245, v245\n"
    "s_nop 0\n"
    "v_cvt_pk_bf16_f32 v197, v244, v245\n"
    "v_sub_f32 v244, v172, v242\n"
    "v_sub_f32 v245, v173, v242\n"
    "v_exp_f32 v244, v244\n"
    "v_exp_f32 v245, v245\n"
    "s_nop 0\n"
    "v_cvt_pk_bf16_f32 v198, v244, v245\n"
    "v_sub_f32 v244, v174, v242\n"
    "v_sub_f32 v245, v175, v242\n"
    "v_exp_f32 v244, v244\n"
    "v_exp_f32 v245, v245\n"
    "s_nop 0\n"
    "v_cvt_pk_bf16_f32 v199, v244, v245\n"
    "s_nop 1\n"
    "v_mfma_f32_16x16x32_bf16 v[220:223], a[240:243], v[192:195], 0\n"
    "v_mfma_f32_16x16x32_bf16 v[220:223], a[240:243], v[196:199], v[220:223]\n"
    "s_nop 7\n"
    "s_nop 3\n"
    "s_cmp_lg_u32 s65, 0\n"
    "s_branch RET4_%=\n"
    ".p2align 6\n"
    "LOOPW_%=:\n"
    "v_mfma_f32_16x16x32_bf16 v[144:147], a[112:115], a[48:51], v[204:207]\n"
    "s_cmp_lt_u32 s42, s43\n"
    "v_exp_f32 v227, v131\n"
    "v_mfma_f32_16x16x32_bf16 v[144:147], a[116:119], a[52:55], v[144:147]\n"
    "s_cselect_b32 s22, 134217728, 0\n"
    "s_add_u32 s45, s47, s41\n"
    "v_cvt_pk_bf16_f32 v177, v226, v227\n"
    "v_mfma_f32_16x16x32_bf16 v[144:147], a[120:123], a[56:59], v[144:147]\n"
    "s_mov_b32 m0, s45\n"
    "v_exp_f32 v224, v132\n"
    "v_mfma_f32_16x16x32_bf16 v[144:147], a[124:127], a[60:63], v[144:147]\n"
    "buffer_load_dwordx4 v237, s[20:23], s42 offen offset:0 nt lds\n"
    "v_mfma_f32_16x16x32_bf16 v[148:151], a[128:131], a[48:51], v[204:207]\n"
    "s_mov_b32 s26, s22\n"
    "v_exp_f32 v225, v133\n"
    "v_mfma_f32_16x16x32_bf16 v[148:151], a[132:135], a[52:55], v[148:151]\n"
    "s_add_u32 s46, s45, 17408\n"
    "v_cvt_pk_bf16_f32 v178, v224, v225\n"
    "v_mfma_f32_16x16x32_bf16 v[148:151], a[136:139], a[56:59], v[148:151]\n"
    "s_add_u32 m0, m0, 64\n"
    "v_exp_f32 v226, v134\n"
    "v_mfma_f32_16x16x32_bf16 v[148:151], a[140:143], a[60:63], v[148:151]\n"
    "buffer_load_dwordx4 v237, s[20:23], s42 offen offset:1024 nt lds\n"
    "v_mfma_f32_16x16x32_bf16 v[152:155], a[144:147], a[48:51], v[204:207]\n"
    "v_exp_f32 v227, v135\n"
    "v_mfma_f32_16x16x32_bf16 v[152:155], a[148:151], a[52:55], v[152:155]\n"
    "v_add_u32 v234, s47, v236\n"
    "v_cvt_pk_bf16_f32 v179, v226, v227\n"
    "v_mfma_f32_16x16x32_bf16 v[152:155], a[152:155], a[56:59], v[152:155]\n"
    "s_add_u32 m0, m0, 64\n"
    "v_exp_f32 v224, v136\n"
    "v_mfma_f32_16x16x32_bf16 v[152:155], a[156:159], a[60:63], v[152:155]\n"
    "buffer_load_dwordx4 v237, s[20:23], s42 offen offset:2048 nt lds\n"
    "v_mfma_f32_16x16x32_bf16 v[156:159], a[160:163], a[48:51], v[204:207]\n"
    "v_exp_f32 v225, v137\n"
    "v_mfma_f32_16x16x32_bf16 v[156:159], a[164:167], a[52:55], v[156:159]\n"
    "v_cvt_pk_bf16_f32 v180, v224, v225\n"
    "v_mfma_f32_16x16x32_bf16 v[156:159], a[168:171], a[56:59], v[156:159]\n"
    "s_add_u32 m0, m0, 64\n"
    "v_exp_f32 v226, v138\n"
    "v_mfma_f32_16x16x32_bf16 v[156:159], a[172:175], a[60:63], v[156:159]\n"
    "buffer_load_dwordx4 v237, s[20:23], s42 offen offset:3072 nt lds\n"
    "s_nop 0\n"
    "v_exp_f32 v227, v139\n"
    "s_nop 0\n"
    "ds_read_b64_tr_b16 a[176:177], v234 offset:0\n"
    "v_cvt_pk_bf16_f32 v181, v226, v227\n"
    "s_nop 0\n"
    "v_exp_f32 v224, v140\n"
    "s_nop 0\n"
    "ds_read_b64_tr_b16 a[178:179], v234 offset:4352\n"
    "ds_read_b64_tr_b16 a[180:181], v234 offset:8704\n"
    "s_nop 0\n"
    "v_exp_f32 v225, v141\n"
    "s_nop 0\n"
    "ds_read_b64_tr_b16 a[182:183], v234 offset:13056\n"
    "v_cvt_pk_bf16_f32 v182, v224, v225\n"
    "s_nop 0\n"
    "v_exp_f32 v226, v142\n"
    "s_nop 0\n"
    "ds_read_b64_tr_b16 a[184:185], v234 offset:32\n"
    "ds_read_b64_tr_b16 a[186:187], v234 offset:4384\n"
    "s_nop 0\n"
    "v_exp_f32 v227, v143\n"
    "s_nop 0\n"
    "ds_read_b64_tr_b16 a[188:189], v234 offset:8736\n"
    "v_cvt_pk_bf16_f32 v183, v226, v227\n"
    "v_mfma_f32_16x16x32_bf16 v[220:223], a[240:243], v[176:179], 0\n"
    "ds_read_b64_tr_b16 a[190:191], v234 offset:13088\n"
    "ds_read_b64_tr_b16 a[192:193], v234 offset:256\n"
    "v_mfma_f32_16x16x32_bf16 v[220:223], a[240:243], v[180:183], v[220:223]\n"
    "ds_read_b64_tr_b16 a[194:195], v234 offset:4608\n"
    "ds_read_b64_tr_b16 a[196:197], v234 offset:8960\n"
    "s_nop 0\n"
    "v_exp_f32 v224, v144\n"
    "s_nop 0\n"
    "ds_read_b64_tr_b16 a[198:199], v234 offset:13312\n"
    "ds_read_b64_tr_b16 a[200:201], v234 offset:288\n"
    "s_nop 0\n"
    "v_exp_f32 v225, v145\n"
    "s_nop 0\n"
    "ds_read_b64_tr_b16 a[202:203], v234 offset:4640\n"
    "v_cmp_nge_f32 vcc, s53, v220\n"
    "s_nop 0\n"
    "v_exp_f32 v226, v146\n"
    "s_nop 0\n"
    "s_cbranch_vccnz RARE0_%=\n"
    "RET0W_%=:\n"
    "ds_read_b64_tr_b16 a[204:205], v234 offset:8992\n"
    "v_cvt_pk_bf16_f32 v184, v224, v225\n"
    "v_mfma_f32_16x16x32_bf16 v[128:131], a[112:115], a[64:67], v[208:211]\n"
    "v_exp_f32 v227, v147\n"
    "v_mfma_f32_16x16x32_bf16 v[128:131], a[116:119], a[68:71], v[128:131]\n"
    "ds_read_b64_tr_b16 a[206:207], v234 offset:13344\n"
    "v_cvt_pk_bf16_f32 v185, v226, v227\n"
    "v_mfma_f32_16x16x32_bf16 v[128:131], a[120:123], a[72:75], v[128:131]\n"
    "v_exp_f32 v224, v148\n"
    "v_mfma_f32_16x16x32_bf16 v[128:131], a[124:127], a[76:79], v[128:131]\n"
    "ds_read_b64_tr_b16 a[208:209], v234 offset:512\n"
    "v_add_f32 v228, v228, v220\n"
    "v_mfma_f32_16x16x32_bf16 v[132:135], a[128:131], a[64:67], v[208:211]\n"
    "v_exp_f32 v225, v149\n"
    "v_mfma_f32_16x16x32_bf16 v[132:135], a[132:135], a[68:71], v[132:135]\n"
    "ds_read_b64_tr_b16 a[210:211], v234 offset:4864\n"
    "v_cvt_pk_bf16_f32 v186, v224, v225\n"
    "v_mfma_f32_16x16x32_bf16 v[132:135], a[136:139], a[72:75], v[132:135]\n"
    "v_exp_f32 v226, v150\n"
    "v_mfma_f32_16x16x32_bf16 v[132:135], a[140:143], a[76:79], v[132:135]\n"
    "ds_read_b64_tr_b16 a[212:213], v234 offset:9216\n"
    "ds_read_b64_tr_b16 a[214:215], v234 offset:13568\n"
    "v_mfma_f32_16x16x32_bf16 v[136:139], a[144:147], a[64:67], v[208:211]\n"
    "v_exp_f32 v227, v151\n"
    "v_mfma_f32_16x16x32_bf16 v[136:139], a[148:151], a[68:71], v[136:139]\n"
    "ds_read_b64_tr_b16 a[216:217], v234 offset:544\n"
    "v_cvt_pk_bf16_f32 v187, v226, v227\n"
    "v_mfma_f32_16x16x32_bf16 v[136:139], a[152:155], a[72:75], v[136:139]\n"
    "v_exp_f32 v224, v152\n"
    "v_mfma_f32_16x16x32_bf16 v[136:139], a[156:159], a[76:79], v[136:139]\n"
    "ds_read_b64_tr_b16 a[218:219], v234 offset:4896\n"
    "ds_read_b64_tr_b16 a[220:221], v234 offset:9248\n"
    "v_mfma_f32_16x16x32_bf16 v[140:143], a[160:163], a[64:67], v[208:211]\n"
    "v_exp_f32 v225, v153\n"
    "v_mfma_f32_16x16x32_bf16 v[140:143], a[164:167], a[68:71], v[140:143]\n"
    "ds_read_b64_tr_b16 a[222:223], v234 offset:13600\n"
    "v_cvt_pk_bf16_f32 v188, v224, v225\n"
    "v_mfma_f32_16x16x32_bf16 v[140:143], a[168:171], a[72:75], v[140:143]\n"
    "v_exp_f32 v226, v154\n"
    "v_mfma_f32_16x16x32_bf16 v[140:143], a[172:175], a[76:79], v[140:143]\n"
    "s_waitcnt lgkmcnt(15)\n"
    "ds_read_b64_tr_b16 a[224:225], v234 offset:768\n"
    "ds_read_b64_tr_b16 a[226:227], v234 offset:5120\n"
    "v_mfma_f32_16x16x32_bf16 v[0:3], a[176:179], v[176:179], v[0:3]\n"
    "v_exp_f32 v227, v155\n"
    "v_mfma_f32_16x16x32_bf16 v[0:3], a[180:183], v[180:183], v[0:3]\n"
    "ds_read_b64_tr_b16 a[228:229], v234 offset:9472\n"
    "v_cvt_pk_bf16_f32 v189, v226, v227\n"
    "v_mfma_f32_16x16x32_bf16 v[4:7], a[184:187], v[176:179], v[4:7]\n"
    "v_exp_f32 v224, v156\n"
    "v_mfma_f32_16x16x32_bf16 v[4:7], a[188:191], v[180:183], v[4:7]\n"
    "s_waitcnt lgkmcnt(11)\n"
    "ds_read_b64_tr_b16 a[230:231], v234 offset:13824\n"
    "ds_read_b64_tr_b16 a[232:233], v234 offset:800\n"
    "v_mfma_f32_16x16x32_bf16 v[8:11], a[192:195], v[176:179], v[8:11]\n"
    "v_exp_f32 v225, v157\n"
    "v_mfma_f32_16x16x32_bf16 v[8:11], a[196:199], v[180:183], v[8:11]\n"
    "ds_read_b64_tr_b16 a[234:235], v234 offset:5152\n"
    "v_cvt_pk_bf16_f32 v190, v224, v225\n"
    "v_mfma_f32_16x16x32_bf16 v[12:15], a[200:203], v[176:179], v[12:15]\n"
    "v_exp_f32 v226, v158\n"
    "v_mfma_f32_16x16x32_bf16 v[12:15], a[204:207], v[180:183], v[12:15]\n"
    "s_waitcnt lgkmcnt(10)\n"
    "ds_read_b64_tr_b16 a[236:237], v234 offset:9504\n"
    "ds_read_b64_tr_b16 a[238:239], v234 offset:13856\n"
    "v_mfma_f32_16x16x32_bf16 v[16:19], a[208:211], v[176:179], v[16:19]\n"
    "v_exp_f32 v227, v159\n"
    "v_mfma_f32_16x16x32_bf16 v[16:19], a[212:215], v[180:183], v[16:19]\n"
    "v_cvt_pk_bf16_f32 v191, v226, v227\n"
    "v_mfma_f32_16x16x32_bf16 v[220:223], a[240:243], v[184:187], 0\n"
    "s_nop 0\n"
    "v_mfma_f32_16x16x32_bf16 v[220:223], a[240:243], v[188:191], v[220:223]\n"
    "s_waitcnt lgkmcnt(8)\n"
    "v_add_u32 v253, 1, v253\n"
    "v_mfma_f32_16x16x32_bf16 v[20:23], a[216:219], v[176:179], v[20:23]\n"
    "v_exp_f32 v224, v128\n"
    "v_mfma_f32_16x16x32_bf16 v[20:23], a[220:223], v[180:183], v[20:23]\n"
    "s_waitcnt lgkmcnt(4)\n"
    "v_mfma_f32_16x16x32_bf16 v[24:27], a[224:227], v[176:179], v[24:27]\n"
    "v_exp_f32 v225, v129\n"
    "v_mfma_f32_16x16x32_bf16 v[24:27], a[228:231], v[180:183], v[24:27]\n"
    "s_waitcnt lgkmcnt(0)\n"
    "v_cmp_nge_f32 vcc, s53, v220\n"
    "v_mfma_f32_16x16x32_bf16 v[28:31], a[232:235], v[176:179], v[28:31]\n"
    "v_exp_f32 v226, v130\n"
    "v_mfma_f32_16x16x32_bf16 v[28:31], a[236:239], v[180:183], v[28:31]\n"
    "s_cbranch_vccnz RARE1_%=\n"
    "RET1W_%=:\n"
    "v_cvt_pk_bf16_f32 v176, v224, v225\n"
    "v_mfma_f32_16x16x32_bf16 v[144:147], a[112:115], a[80:83], v[212:215]\n"
    "s_waitcnt vmcnt(12) lgkmcnt(0)\n"
    "v_exp_f32 v227, v131\n"
    "v_mfma_f32_16x16x32_bf16 v[144:147], a[116:119], a[84:87], v[144:147]\n"
    "ds_write_b32 v254, v253 offset:0\n"
    "v_cvt_pk_bf16_f32 v177, v226, v227\n"
    "v_mfma_f32_16x16x32_bf16 v[144:147], a[120:123], a[88:91], v[144:147]\n"
    "v_exp_f32 v224, v132\n"
    "v_mfma_f32_16x16x32_bf16 v[144:147], a[124:127], a[92:95], v[144:147]\n"
    "v_add_f32 v229, v229, v220\n"
    "v_mfma_f32_16x16x32_bf16 v[148:151], a[128:131], a[80:83], v[212:215]\n"
    "v_exp_f32 v225, v133\n"
    "v_mfma_f32_16x16x32_bf16 v[148:151], a[132:135], a[84:87], v[148:151]\n"
    "v_cvt_pk_bf16_f32 v178, v224, v225\n"
    "v_mfma_f32_16x16x32_bf16 v[148:151], a[136:139], a[88:91], v[148:151]\n"
    "v_exp_f32 v226, v134\n"
    "v_mfma_f32_16x16x32_bf16 v[148:151], a[140:143], a[92:95], v[148:151]\n"
    "v_mfma_f32_16x16x32_bf16 v[152:155], a[144:147], a[80:83], v[212:215]\n"
    "v_exp_f32 v227, v135\n"
    "v_mfma_f32_16x16x32_bf16 v[152:155], a[148:151], a[84:87], v[152:155]\n"
    "ds_read_b32 v252, v255 offset:0\n"
    "v_cvt_pk_bf16_f32 v179, v226, v227\n"
    "v_mfma_f32_16x16x32_bf16 v[152:155], a[152:155], a[88:91], v[152:155]\n"
    "v_exp_f32 v224, v136\n"
    "v_mfma_f32_16x16x32_bf16 v[152:155], a[156:159], a[92:95], v[152:155]\n"
    "v_mfma_f32_16x16x32_bf16 v[156:159], a[160:163], a[80:83], v[212:215]\n"
    "v_exp_f32 v225, v137\n"
    "v_mfma_f32_16x16x32_bf16 v[156:159], a[164:167], a[84:87], v[156:159]\n"
    "v_cvt_pk_bf16_f32 v180, v224, v225\n"
    "v_mfma_f32_16x16x32_bf16 v[156:159], a[168:171], a[88:91], v[156:159]\n"
    "v_exp_f32 v226, v138\n"
    "v_mfma_f32_16x16x32_bf16 v[156:159], a[172:175], a[92:95], v[156:159]\n"
    "s_waitcnt lgkmcnt(0)\n"
    "v_cmp_gt_u32 vcc, v253, v252\n"
    "v_mfma_f32_16x16x32_bf16 v[32:35], a[176:179], v[184:187], v[32:35]\n"
    "v_exp_f32 v227, v139\n"
    "v_mfma_f32_16x16x32_bf16 v[32:35], a[180:183], v[188:191], v[32:35]\n"
    "s_cbranch_vccnz SPIN1_%=\n"
    "SPIN1_RETW_%=:\n"
    "v_cvt_pk_bf16_f32 v181, v226, v227\n"
    "v_mfma_f32_16x16x32_bf16 v[36:39], a[184:187], v[184:187], v[36:39]\n"
    "s_mov_b32 m0, s46\n"
    "v_exp_f32 v224, v140\n"
    "v_mfma_f32_16x16x32_bf16 v[36:39], a[188:191], v[188:191], v[36:39]\n"
    "buffer_load_dwordx4 v238, s[24:27], s42 offen offset:0 nt lds\n"
    "v_mfma_f32_16x16x32_bf16 v[40:43], a[192:195], v[184:187], v[40:43]\n"
    "v_exp_f32 v225, v141\n"
    "v_mfma_f32_16x16x32_bf16 v[40:43], a[196:199], v[188:191], v[40:43]\n"
    "v_cvt_pk_bf16_f32 v182, v224, v225\n"
    "v_mfma_f32_16x16x32_bf16 v[44:47], a[200:203], v[184:187], v[44:47]\n"
    "s_add_u32 m0, m0, 32\n"
    "v_exp_f32 v226, v142\n"
    "v_mfma_f32_16x16x32_bf16 v[44:47], a[204:207], v[188:191], v[44:47]\n"
    "buffer_load_dwordx4 v238, s[24:27], s42 offen offset:1024 nt lds\n"
    "v_mfma_f32_16x16x32_bf16 v[48:51], a[208:211], v[184:187], v[48:51]\n"
    "v_exp_f32 v227, v143\n"
    "v_mfma_f32_16x16x32_bf16 v[48:51], a[212:215], v[188:191], v[48:51]\n"
    "v_cvt_pk_bf16_f32 v183, v226, v227\n"
    "v_mfma_f32_16x16x32_bf16 v[220:223], a[240:243], v[176:179], 0\n"
    "s_add_u32 m0, m0, 32\n"
    "v_mfma_f32_16x16x32_bf16 v[220:223], a[240:243], v[180:183], v[220:223]\n"
    "buffer_load_dwordx4 v238, s[24:27], s42 offen offset:2048 nt lds\n"
    "v_mfma_f32_16x16x32_bf16 v[52:55], a[216:219], v[184:187], v[52:55]\n"
    "s_add_u32 m0, m0, 32\n"
    "v_exp_f32 v224, v144\n"
    "v_mfma_f32_16x16x32_bf16 v[52:55], a[220:223], v[188:191], v[52:55]\n"
    "buffer_load_dwordx4 v238, s[24:27], s42 offen offset:3072 nt lds\n"
    "v_mfma_f32_16x16x32_bf16 v[56:59], a[224:227], v[184:187], v[56:59]\n"
    "v_exp_f32 v225, v145\n"
    "v_mfma_f32_16x16x32_bf16 v[56:59], a[228:231], v[188:191], v[56:59]\n"
    "v_cmp_nge_f32 vcc, s53, v220\n"
    "v_mfma_f32_16x16x32_bf16 v[60:63], a[232:235], v[184:187], v[60:63]\n"
    "s_add_u32 s42, s42, 0x4000\n"
    "v_exp_f32 v226, v146\n"
    "v_mfma_f32_16x16x32_bf16 v[60:63], a[236:239], v[188:191], v[60:63]\n"
    "s_cbranch_vccnz RARE2_%=\n"
    "RET2W_%=:\n"
    "v_add_u32 v233, s48, v241\n"
    "v_cvt_pk_bf16_f32 v184, v224, v225\n"
    "s_nop 0\n"
    "v_exp_f32 v227, v147\n"
    "s_nop 0\n"
    "ds_read_b128 a[112:115], v233 offset:0\n"
    "v_cvt_pk_bf16_f32 v185, v226, v227\n"
    "s_nop 0\n"
    "v_exp_f32 v224, v148\n"
    "s_nop 0\n"
    "ds_read_b128 a[116:119], v233 offset:64\n"
    "v_add_f32 v230, v230, v220\n"
    "s_nop 0\n"
    "v_exp_f32 v225, v149\n"
    "s_nop 0\n"
    "ds_read_b128 a[120:123], v233 offset:128\n"
    "v_cvt_pk_bf16_f32 v186, v224, v225\n"
    "s_nop 0\n"
    "v_exp_f32 v226, v150\n"
    "s_nop 0\n"
    "ds_read_b128 a[124:127], v233 offset:192\n"
    "ds_read_b128 a[128:131], v233 offset:4352\n"
    "s_nop 0\n"
    "v_exp_f32 v227, v151\n"
    "s_nop 0\n"
    "ds_read_b128 a[132:135], v233 offset:4416\n"
    "v_cvt_pk_bf16_f32 v187, v226, v227\n"
    "s_nop 0\n"
    "v_exp_f32 v224, v152\n"
    "s_nop 0\n"
    "ds_read_b128 a[136:139], v233 offset:4480\n"
    "ds_read_b128 a[140:143], v233 offset:4544\n"
    "s_nop 0\n"
    "v_exp_f32 v225, v153\n"
    "s_nop 0\n"
    "ds_read_b128 a[144:147], v233 offset:8704\n"
    "v_cvt_pk_bf16_f32 v188, v224, v225\n"
    "s_nop 0\n"
    "v_exp_f32 v226, v154\n"
    "s_nop 0\n"
    "ds_read_b128 a[148:151], v233 offset:8768\n"
    "ds_read_b128 a[152:155], v233 offset:8832\n"
    "v_mfma_f32_16x16x32_bf16 v[64:67], a[176:179], v[176:179], v[64:67]\n"
    "v_exp_f32 v227, v155\n"
    "v_mfma_f32_16x16x32_bf16 v[64:67], a[180:183], v[180:183], v[64:67]\n"
    "ds_read_b128 a[156:159], v233 offset:8896\n"
    "v_cvt_pk_bf16_f32 v189, v226, v227\n"
    "v_mfma_f32_16x16x32_bf16 v[68:71], a[184:187], v[176:179], v[68:71]\n"
    "v_exp_f32 v224, v156\n"
    "v_mfma_f32_16x16x32_bf16 v[68:71], a[188:191], v[180:183], v[68:71]\n"
    "ds_read_b128 a[160:163], v233 offset:13056\n"
    "ds_read_b128 a[164:167], v233 offset:13120\n"
    "v_mfma_f32_16x16x32_bf16 v[72:75], a[192:195], v[176:179], v[72:75]\n"
    "v_exp_f32 v225, v157\n"
    "v_mfma_f32_16x16x32_bf16 v[72:75], a[196:199], v[180:183], v[72:75]\n"
    "ds_read_b128 a[168:171], v233 offset:13184\n"
    "v_cvt_pk_bf16_f32 v190, v224, v225\n"
    "v_mfma_f32_16x16x32_bf16 v[76:79], a[200:203], v[176:179], v[76:79]\n"
    "v_exp_f32 v226, v158\n"
    "v_mfma_f32_16x16x32_bf16 v[76:79], a[204:207], v[180:183], v[76:79]\n"
    "ds_read_b128 a[172:175], v233 offset:13248\n"
    "v_mfma_f32_16x16x32_bf16 v[80:83], a[208:211], v[176:179], v[80:83]\n"
    "v_exp_f32 v227, v159\n"
    "v_mfma_f32_16x16x32_bf16 v[80:83], a[212:215], v[180:183], v[80:83]\n"
    "v_cvt_pk_bf16_f32 v191, v226, v227\n"
    "v_mfma_f32_16x16x32_bf16 v[220:223], a[240:243], v[184:187], 0\n"
    "s_nop 0\n"
    "v_mfma_f32_16x16x32_bf16 v[220:223], a[240:243], v[188:191], v[220:223]\n"
    "v_mfma_f32_16x16x32_bf16 v[84:87], a[216:219], v[176:179], v[84:87]\n"
    "s_nop 0\n"
    "v_mfma_f32_16x16x32_bf16 v[84:87], a[220:223], v[180:183], v[84:87]\n"
    "s_nop 0\n"
    "v_add_u32 v253, 1, v253\n"
    "v_mfma_f32_16x16x32_bf16 v[88:91], a[224:227], v[176:179], v[88:91]\n"
    "s_nop 0\n"
    "v_mfma_f32_16x16x32_bf16 v[88:91], a[228:231], v[180:183], v[88:91]\n"
    "v_cmp_nge_f32 vcc, s53, v220\n"
    "v_mfma_f32_16x16x32_bf16 v[92:95], a[232:235], v[176:179], v[92:95]\n"
    "v_mfma_f32_16x16x32_bf16 v[92:95], a[236:239], v[180:183], v[92:95]\n"
    "s_waitcnt lgkmcnt(0)\n"
    "s_cbranch_vccnz RARE3_%=\n"
    "RET3W_%=:\n"
    "v_mfma_f32_16x16x32_bf16 v[128:131], a[112:115], a[32:35], v[200:203]\n"
    "s_waitcnt vmcnt(12)\n"
    "v_mfma_f32_16x16x32_bf16 v[128:131], a[116:119], a[36:39], v[128:131]\n"
    "s_bitcmp1_b32 s61, s51\n"
    "ds_write_b32 v254, v253 offset:0\n"
    "v_mfma_f32_16x16x32_bf16 v[128:131], a[120:123], a[40:43], v[128:131]\n"
    "s_cselect_b32 s38, s57, 0\n"
    "v_mfma_f32_16x16x32_bf16 v[128:131], a[124:127], a[44:47], v[128:131]\n"
    "s_cselect_b32 s30, s60, 0\n"
    "v_add_u32 v252, s55, v239\n"
    "v_mfma_f32_16x16x32_bf16 v[132:135], a[128:131], a[32:35], v[200:203]\n"
    "s_cselect_b32 m0, s49, s56\n"
    "v_mfma_f32_16x16x32_bf16 v[132:135], a[132:135], a[36:39], v[132:135]\n"
    "s_cselect_b32 s68, 2048, 0\n"
    "v_add_f32 v231, v231, v220\n"
    "v_mfma_f32_16x16x32_bf16 v[132:135], a[136:139], a[40:43], v[132:135]\n"
    "s_cselect_b32 s67, s50, 0\n"
    "v_mfma_f32_16x16x32_bf16 v[132:135], a[140:143], a[44:47], v[132:135]\n"
    "s_cselect_b32 s66, 2176, 0\n"
    "ds_read_b128 v[244:247], v252 offset:0\n"
    "ds_read_b128 v[248:251], v252 offset:1088\n"
    "v_mfma_f32_16x16x32_bf16 v[136:139], a[144:147], a[32:35], v[200:203]\n"
    "v_mfma_f32_16x16x32_bf16 v[136:139], a[148:151], a[36:39], v[136:139]\n"
    "v_mfma_f32_16x16x32_bf16 v[136:139], a[152:155], a[40:43], v[136:139]\n"
    "v_mfma_f32_16x16x32_bf16 v[136:139], a[156:159], a[44:47], v[136:139]\n"
    "buffer_load_dwordx4 v237, s[36:39], s44 offen offset:0 nt lds\n"
    "v_mfma_f32_16x16x32_bf16 v[140:143], a[160:163], a[32:35], v[200:203]\n"
    "s_add_u32 m0, m0, 64\n"
    "v_mfma_f32_16x16x32_bf16 v[140:143], a[164:167], a[36:39], v[140:143]\n"
    "ds_read_b32 v252, v255 offset:0\n"
    "v_mfma_f32_16x16x32_bf16 v[140:143], a[168:171], a[40:43], v[140:143]\n"
    "v_mfma_f32_16x16x32_bf16 v[140:143], a[172:175], a[44:47], v[140:143]\n"
    "buffer_load_dwordx4 v237, s[36:39], s44 offen offset:1024 nt lds\n"
    "v_mfma_f32_16x16x32_bf16 v[96:99], a[176:179], v[184:187], v[96:99]\n"
    "s_waitcnt lgkmcnt(1)\n"
    "v_mfma_f32_16x16x32_bf16 v[96:99], a[180:183], v[188:191], v[96:99]\n"
    "v_mfma_f32_16x16x32_bf16 v[100:103], a[184:187], v[184:187], v[100:103]\n"
    "v_mfma_f32_16x16x32_bf16 v[100:103], a[188:191], v[188:191], v[100:103]\n"
    "buffer_store_dwordx4 v[244:247], v240, s[28:31], s44 offen offset:0 nt\n"
    "v_mfma_f32_16x16x32_bf16 v[104:107], a[192:195], v[184:187], v[104:107]\n"
    "v_mfma_f32_16x16x32_bf16 v[104:107], a[196:199], v[188:191], v[104:107]\n"
    "s_waitcnt lgkmcnt(0)\n"
    "v_cmp_gt_u32 vcc, v253, v252\n"
    "v_mfma_f32_16x16x32_bf16 v[108:111], a[200:203], v[184:187], v[108:111]\n"
    "v_mfma_f32_16x16x32_bf16 v[108:111], a[204:207], v[188:191], v[108:111]\n"
    "s_cbranch_vccnz SPIN2_%=\n"
    "SPIN2_RETW_%=:\n"
    "buffer_store_dwordx4 v[248:251], v240, s[28:31], s44 offen offset:1024 nt\n"
    "v_mfma_f32_16x16x32_bf16 v[112:115], a[208:211], v[184:187], v[112:115]\n"
    "s_add_u32 s44, s44, s68\n"
    "v_mfma_f32_16x16x32_bf16 v[112:115], a[212:215], v[188:191], v[112:115]\n"
    "s_add_u32 s49, s49, s67\n"
    "s_nop 0\n"
    "s_add_u32 s55, s55, s66\n"
    "s_nop 0\n"
    "s_xor_b32 s47, s47, 34816\n"
    "v_mfma_f32_16x16x32_bf16 v[116:119], a[216:219], v[184:187], v[116:119]\n"
    "s_xor_b32 s48, s48, 34816\n"
    "v_exp_f32 v224, v128\n"
    "v_mfma_f32_16x16x32_bf16 v[116:119], a[220:223], v[188:191], v[116:119]\n"
    "s_add_u32 s51, s51, 1\n"
    "v_mfma_f32_16x16x32_bf16 v[120:123], a[224:227], v[184:187], v[120:123]\n"
    "s_cmp_lt_u32 s51, 16\n"
    "v_exp_f32 v225, v129\n"
    "v_mfma_f32_16x16x32_bf16 v[120:123], a[228:231], v[188:191], v[120:123]\n"
    "v_mfma_f32_16x16x32_bf16 v[124:127], a[232:235], v[184:187], v[124:127]\n"
    "v_exp_f32 v226, v130\n"
    "v_mfma_f32_16x16x32_bf16 v[124:127], a[236:239], v[188:191], v[124:127]\n"
    "v_cvt_pk_bf16_f32 v176, v224, v225\n"
    "s_cbranch_scc1 LOOPW_%=\n"
    "s_branch POST_%=\n"
    "SPIN1_%=:\n"
    "ds_read_b32 v252, v255 offset:0\n"
    "s_waitcnt lgkmcnt(0)\n"
    "v_cmp_gt_u32 vcc, v253, v252\n"
    "s_nop 1\n"
    "s_cbranch_vccnz SPIN1S_%=\n"
    "s_cselect_b32 s65, 1, 0\n"
    "s_cmp_eq_u32 s40, 3\n"
    "s_cbranch_scc1 SPIN1X_%=\n"
    "s_cmp_lg_u32 s65, 0\n"
    "s_branch SPIN1_RET_%=\n"
    "SPIN1X_%=:\n"
    "s_cmp_lg_u32 s65, 0\n"
    "s_branch SPIN1_RETW_%=\n"
    "SPIN1S_%=:\n"
    "s_sleep 1\n"
    "s_branch SPIN1_%=\n"
    "SPIN2_%=:\n"
    "ds_read_b32 v252, v255 offset:0\n"
    "s_waitcnt lgkmcnt(0)\n"
    "v_cmp_gt_u32 vcc, v253, v252\n"
    "s_nop 1\n"
    "s_cbranch_vccnz SPIN2S_%=\n"
    "s_cselect_b32 s65, 1, 0\n"
    "s_cmp_eq_u32 s40, 3\n"
    "s_cbranch_scc1 SPIN2X_%=\n"
    "s_cmp_lg_u32 s65, 0\n"
    "s_branch SPIN2_RET_%=\n"
    "SPIN2X_%=:\n"
    "s_cmp_lg_u32 s65, 0\n"
    "s_branch SPIN2_RETW_%=\n"
    "SPIN2S_%=:\n"
    "s_sleep 1\n"
    "s_branch SPIN2_%=\n"
    "END_%=:\n"
    :: "s"(q), "s"(k), "s"(v), "s"(o), "s"(bid), "s"(w), "s"(ldsb)
    : "v0", "v1", "v2", "v3", "v4", "v5", "v6", "v7", "v8", "v9", "v10", "v11", "v12", "v13", "v14", "v15", "v16", "v17", "v18", "v19", "v20", "v21", "v22", "v23", "v24", "v25", "v26", "v27", "v28", "v29", "v30", "v31", "v32", "v33", "v34", "v35", "v36", "v37", "v38", "v39", "v40", "v41", "v42", "v43", "v44", "v45", "v46", "v47", "v48", "v49", "v50", "v51", "v52", "v53", "v54", "v55", "v56", "v57", "v58", "v59", "v60", "v61", "v62", "v63", "v64", "v65", "v66", "v67", "v68", "v69", "v70", "v71", "v72", "v73", "v74", "v75", "v76", "v77", "v78", "v79", "v80", "v81", "v82", "v83", "v84", "v85", "v86", "v87", "v88", "v89", "v90", "v91", "v92", "v93", "v94", "v95", "v96", "v97", "v98", "v99", "v100", "v101", "v102", "v103", "v104", "v105", "v106", "v107", "v108", "v109", "v110", "v111", "v112", "v113", "v114", "v115", "v116", "v117", "v118", "v119", "v120", "v121", "v122", "v123", "v124", "v125", "v126", "v127", "v128", "v129", "v130", "v131", "v132", "v133", "v134", "v135", "v136", "v137", "v138", "v139", "v140", "v141", "v142", "v143", "v144", "v145", "v146", "v147", "v148", "v149", "v150", "v151", "v152", "v153", "v154", "v155", "v156", "v157", "v158", "v159", "v160", "v161", "v162", "v163", "v164", "v165", "v166", "v167", "v168", "v169", "v170", "v171", "v172", "v173", "v174", "v175", "v176", "v177", "v178", "v179", "v180", "v181", "v182", "v183", "v184", "v185", "v186", "v187", "v188", "v189", "v190", "v191", "v192", "v193", "v194", "v195", "v196", "v197", "v198", "v199", "v200", "v201", "v202", "v203", "v204", "v205", "v206", "v207", "v208", "v209", "v210", "v211", "v212", "v213", "v214", "v215", "v216", "v217", "v218", "v219", "v220", "v221", "v222", "v223", "v224", "v225", "v226", "v227", "v228", "v229", "v230", "v231", "v232", "v233", "v234", "v235", "v236", "v237", "v238", "v239", "v240", "v241", "v242", "v243", "v244", "v245", "v246", "v247", "v248", "v249", "v250", "v251", "v252", "v253", "v254", "v255", "a0", "a1", "a2", "a3", "a4", "a5", "a6", "a7", "a8", "a9", "a10", "a11", "a12", "a13", "a14", "a15", "a16", "a17", "a18", "a19", "a20", "a21", "a22", "a23", "a24", "a25", "a26", "a27", "a28", "a29", "a30", "a31", "a32", "a33", "a34", "a35", "a36", "a37", "a38", "a39", "a40", "a41", "a42", "a43", "a44", "a45", "a46", "a47", "a48", "a49", "a50", "a51", "a52", "a53", "a54", "a55", "a56", "a57", "a58", "a59", "a60", "a61", "a62", "a63", "a64", "a65", "a66", "a67", "a68", "a69", "a70", "a71", "a72", "a73", "a74", "a75", "a76", "a77", "a78", "a79", "a80", "a81", "a82", "a83", "a84", "a85", "a86", "a87", "a88", "a89", "a90", "a91", "a92", "a93", "a94", "a95", "a96", "a97", "a98", "a99", "a100", "a101", "a102", "a103", "a104", "a105", "a106", "a107", "a108", "a109", "a110", "a111", "a112", "a113", "a114", "a115", "a116", "a117", "a118", "a119", "a120", "a121", "a122", "a123", "a124", "a125", "a126", "a127", "a128", "a129", "a130", "a131", "a132", "a133", "a134", "a135", "a136", "a137", "a138", "a139", "a140", "a141", "a142", "a143", "a144", "a145", "a146", "a147", "a148", "a149", "a150", "a151", "a152", "a153", "a154", "a155", "a156", "a157", "a158", "a159", "a160", "a161", "a162", "a163", "a164", "a165", "a166", "a167", "a168", "a169", "a170", "a171", "a172", "a173", "a174", "a175", "a176", "a177", "a178", "a179", "a180", "a181", "a182", "a183", "a184", "a185", "a186", "a187", "a188", "a189", "a190", "a191", "a192", "a193", "a194", "a195", "a196", "a197", "a198", "a199", "a200", "a201", "a202", "a203", "a204", "a205", "a206", "a207", "a208", "a209", "a210", "a211", "a212", "a213", "a214", "a215", "a216", "a217", "a218", "a219", "a220", "a221", "a222", "a223", "a224", "a225", "a226", "a227", "a228", "a229", "a230", "a231", "a232", "a233", "a234", "a235", "a236", "a237", "a238", "a239", "a240", "a241", "a242", "a243", "a244", "a245", "a246", "a247", "a248", "a249", "a250", "a251", "a252", "a253", "a254", "a255", "s20", "s21", "s22", "s23", "s24", "s25", "s26", "s27", "s28", "s29", "s30", "s31", "s34", "s35", "s36", "s37", "s38", "s39", "s40", "s41", "s42", "s43", "s44", "s45", "s46", "s47", "s48", "s49", "s50", "s51", "s52", "s53", "s54", "s55", "s56", "s57", "s58", "s59", "s60", "s61", "s62", "s63", "s64", "s65", "s66", "s67", "s68", "s69", "s70", "s71", "s72", "s73", "s74", "s75", "s76", "s77", "s78", "s79", "s80", "s81", "s82", "s83", "s84", "s85", "s86", "s87", "s88", "s89", "s90", "s91", "s92", "s93", "s94", "s95", "vcc", "scc", "memory");
}

extern "C" void kernel_launch(void* const* d_in, const int* in_sizes, int n_in, void* d_out, int out_size, void* d_ws, size_t ws_size,
                              hipStream_t stream) {
  mha_fwd_kernel<<<256, 256, 0, stream>>>(d_in[0], d_in[1], d_in[2], d_out);
}
```
